```python
import torch

def _softmax(x, axis=-1):
    return torch.softmax(x, dim=axis)
def _gelu(x):
    return torch.nn.functional.gelu(x, approximate="tanh")
def _silu(x):
    return torch.nn.functional.silu(x)
def _sigmoid(x):
    return torch.sigmoid(x)
def _layernorm(x, axis=-1, eps=1e-5):
    m = x.mean(dim=axis, keepdim=True)
    v = x.var(dim=axis, keepdim=True, unbiased=False)
    return (x - m) / torch.sqrt(v + eps)
def _scatter_add(tgt, idx, src, axis=0):
    return tgt.clone().index_add_(axis, idx, src)
def _segment_sum(src, seg, n):
    out = torch.zeros((n,) + src.shape[1:], dtype=src.dtype)
    return out.index_add_(0, seg, src)
def _pad(x, pw):
    flat = tuple(v for pair in reversed(pw) for v in pair)
    return torch.nn.functional.pad(x, flat)

kernel_name = 'concat_heads__sliding_block__softmax_full_mha__prompt'

def setup_inputs():

    _w = (lambda a: (lambda t: t.to(torch.bfloat16) if t.is_floating_point() else t)(torch.as_tensor(a)))
    def _A_setup():
        B, S, D = 1, 2048, 448
        return {
            "x": _w((lambda *s: torch.randn(*s, dtype=torch.bfloat16) * 0.2)(B, S, D)),
            "wqkv": _w((lambda *s: torch.randn(*s, dtype=torch.bfloat16) * 0.8)(D, 3 * D)),
            "wo": _w((lambda *s: torch.randn(*s, dtype=torch.bfloat16) * 3.2)(D, D)),
        }
    _A_in = _A_setup()

    def _B_setup():
        B, S, D = 1, 2048, 448
        return {
            "x": _w((lambda *s: torch.randn(*s, dtype=torch.bfloat16) * 0.2)(B, S, D)),
            "wqkv": _w((lambda *s: torch.randn(*s, dtype=torch.bfloat16) * 0.8)(D, 3 * D)),
            "wo": _w((lambda *s: torch.randn(*s, dtype=torch.bfloat16) * 3.2)(D, D)),
        }
    _B_in = _B_setup()

    return {
        "A_x": _A_in["x"],
        "A_wqkv": _A_in["wqkv"],
        "A_wo": _A_in["wo"],
        "B_x": _B_in["x"],
        "B_wqkv": _B_in["wqkv"],
        "B_wo": _B_in["wo"],
    }

def reference(A_x, A_wqkv, A_wo, B_x, B_wqkv, B_wo):
    def _A_ref(x, wqkv, wo):
        B, S, D = x.shape
        H = 16; Dh = D // H; W = 256
        qkv = (lambda x, *d: x.permute(*d))((x @ wqkv).reshape(B, S, 3, H, Dh), 2, 0, 3, 1, 4)
        q, k, v = qkv[0], qkv[1], qkv[2]
        scores = torch.einsum("bhqd,bhkd->bhqk", q, k) * (Dh ** -0.5)
        idx = torch.arange(S)
        mask = (idx[:, None] - idx[None, :] >= 0) & (idx[:, None] - idx[None, :] < W)
        scores = torch.where(mask[None, None], scores, -1e9)
        p = _softmax(scores, axis=-1)
        o = torch.einsum("bhqk,bhkd->bhqd", p, v)
        return (lambda x, *d: x.permute(*d))(o, 0, 2, 1, 3).reshape(B, S, D) @ wo

    def _B_ref(x, wqkv, wo):
        B, S, D = x.shape
        H = 16; Dh = D // H
        qkv = (lambda x, *d: x.permute(*d))((x @ wqkv).reshape(B, S, 3, H, Dh), 2, 0, 3, 1, 4)
        q, k, v = qkv[0], qkv[1], qkv[2]
        scale = Dh ** -0.5
        scores = torch.einsum("bhqd,bhkd->bhqk", q, k) * scale
        p = _softmax(scores, axis=-1)
        o = torch.einsum("bhqk,bhkd->bhqd", p, v)
        return (lambda x, *d: x.permute(*d))(o, 0, 2, 1, 3).reshape(B, S, D) @ wo

    a = _A_ref(A_x, A_wqkv, A_wo)
    b = _B_ref(B_x, B_wqkv, B_wo)
    return torch.concatenate([a, b], axis=-1)
```

```cpp
#include <hip/hip_runtime.h>
#include <hip/hip_bf16.h>
#include <stdint.h>

typedef __bf16 bf16x8 __attribute__((ext_vector_type(8)));
typedef __bf16 bf16x2 __attribute__((ext_vector_type(2)));
typedef float f32x16 __attribute__((ext_vector_type(16)));
typedef float f32x2 __attribute__((ext_vector_type(2)));
typedef unsigned u32x4 __attribute__((ext_vector_type(4)));
typedef unsigned u32x2 __attribute__((ext_vector_type(2)));
typedef short s16x4 __attribute__((ext_vector_type(4)));
typedef short s16x8 __attribute__((ext_vector_type(8)));
#define LAS __attribute__((address_space(3)))
#define GAS __attribute__((address_space(1)))
#define DI __device__ __forceinline__

constexpr int S_ = 2048, D_ = 448, NH = 16, DH = 28;
constexpr unsigned WS_Q = 0;
constexpr unsigned WS_KV = 4u << 20;
constexpr unsigned WS_O = 12u << 20;
constexpr float QSCALE = 0.18898223650461362f * 1.4426950408889634f;
constexpr float MBIAS = 40.f;

DI f32x16 mfma(bf16x8 a, bf16x8 b, f32x16 c) { return __builtin_amdgcn_mfma_f32_32x32x16_bf16(a, b, c, 0, 0, 0); }
DI unsigned cvtpk(float lo, float hi) { f32x2 v = {lo, hi}; bf16x2 b = __builtin_convertvector(v, bf16x2); return __builtin_bit_cast(unsigned, b); }
DI unsigned rfl(unsigned x) { return __builtin_amdgcn_readfirstlane(x); }
DI __amdgpu_buffer_rsrc_t mk_rsrc(const void* p, unsigned bytes) { return __builtin_amdgcn_make_buffer_rsrc((void*)p, (short)0, (int)bytes, 0x00020000); }
DI void dma16(unsigned lds_dst, unsigned voff, __amdgpu_buffer_rsrc_t rsrc, unsigned soff) {
    asm volatile("s_mov_b32 m0, %0\n\ts_nop 0\n\tbuffer_load_dwordx4 %1, %2, %3 offen lds" :: "s"(lds_dst), "v"(voff), "s"(rsrc), "s"(soff) : "memory");
}
DI bf16x8 ldsr128(unsigned addr) { return *(const LAS bf16x8*)(size_t)addr; }
typedef short v4i16_t __attribute__((ext_vector_type(4)));
DI s16x4 ldstr(unsigned addr) { return __builtin_amdgcn_ds_read_tr16_b64_v4i16((LAS v4i16_t*)(size_t)addr); }
DI bf16x8 cat8(s16x4 a, s16x4 b) { s16x8 r = {a[0], a[1], a[2], a[3], b[0], b[1], b[2], b[3]}; return __builtin_bit_cast(bf16x8, r); }
#define WAITV(n) asm volatile("s_waitcnt vmcnt(" #n ")" ::: "memory")
#define BAR() asm volatile("s_waitcnt lgkmcnt(0)\n\ts_barrier" ::: "memory")

constexpr int K1_SLOT = 40960, K1_LDS = 3 * K1_SLOT;
__global__ void __launch_bounds__(256, 1) k_qkv(const uint16_t* __restrict__ xA, const uint16_t* __restrict__ wA, const uint16_t* __restrict__ xB,
                                                const uint16_t* __restrict__ wB, char* __restrict__ ws) {
    extern __shared__ __attribute__((aligned(16))) char lds[];
    const unsigned tid = threadIdx.x, lane = tid & 63, wid = rfl(tid >> 6), r = lane & 31, h = lane >> 5;
    const unsigned bid = blockIdx.x, g = bid & 7, j = bid >> 3;
    const unsigned br = g & 1, tb = ((g >> 1) & 1) * 8 + (j & 7), pr = (g >> 2) * 4 + (j >> 3);
    const uint16_t* x = br ? xB : xA; const uint16_t* w = br ? wB : wA;
    const __amdgpu_buffer_rsrc_t rx = mk_rsrc(x, S_ * D_ * 2), rw = mk_rsrc(w, D_ * 3 * D_ * 2);
    const unsigned lds0 = (unsigned)(size_t)lds;
    const unsigned vx0 = (lane >> 3) * 896 + (((lane & 7) ^ ((lane >> 4) & 7)) << 4);
    const unsigned vx1 = (lane >> 3) * 896 + (((lane & 7) ^ ((4 + (lane >> 4)) & 7)) << 4);
    const unsigned vw = (lane >> 3) * 2688 + (((lane & 7) ^ (((lane >> 4) & 1) << 2)) << 4);
    auto issue = [&](int c) {
        const unsigned slot = lds0 + (c % 3) * K1_SLOT;
#pragma unroll
        for (int i = 0; i < 4; ++i) { const unsigned ii = 4 * wid + i;
            dma16(slot + ii * 1024, (i & 1) ? vx1 : vx0, rx, (tb * 128 + 8 * ii) * 896 + c * 128); }
#pragma unroll
        for (int s = 0; s < 3; ++s)
#pragma unroll
            for (int jj = 0; jj < 2; ++jj) { const unsigned rr = 2 * wid + jj;
                dma16(slot + 16384 + s * 8192 + rr * 1024, vw, rw, (c * 64 + 8 * rr) * 2688 + s * 896 + pr * 112); }
    };
    const unsigned tp = wid & 1, hh = wid >> 1;
    unsigned xa[4];
#pragma unroll
    for (int ks = 0; ks < 4; ++ks) xa[ks] = lds0 + (tp * 64 + r) * 128 + ((((2 * ks + h) ^ ((r >> 1) & 7))) << 4);
    const unsigned q4 = (lane & 15) >> 2, p4 = lane & 3, blk = (lane >> 4) & 1;
    const unsigned lb = 56 * hh + 32 * blk + 8 * p4;
    const unsigned wa = lds0 + 16384 + (8 * h + q4) * 128 + ((((lb >> 4) ^ (((q4 >> 1) & 1) << 2)) << 4) | (lb & 15));
    f32x16 acc[2][3];
#pragma unroll
    for (int a = 0; a < 2; ++a)
#pragma unroll
        for (int b = 0; b < 3; ++b) acc[a][b] = f32x16{};
    issue(0); issue(1);
#pragma unroll
    for (int c = 0; c < 7; ++c) {
        if (c < 6) WAITV(10); else WAITV(0);
        BAR();
        if (c + 2 < 7) issue(c + 2);
        const unsigned so = (c % 3) * K1_SLOT;
#pragma unroll
        for (int ks = 0; ks < 4; ++ks) {
            bf16x8 xf[2], wf[3];
#pragma unroll
            for (int a = 0; a < 2; ++a) xf[a] = ldsr128(xa[ks] + so + a * 4096);
#pragma unroll
            for (int s = 0; s < 3; ++s) wf[s] = cat8(ldstr(wa + so + s * 8192 + ks * 2048), ldstr(wa + so + s * 8192 + ks * 2048 + 512));
#pragma unroll
            for (int a = 0; a < 2; ++a) {
                acc[a][0] = mfma(wf[0], xf[a], acc[a][0]);
                acc[a][1] = mfma(wf[1], xf[a], acc[a][1]);
                acc[a][2] = mfma(xf[a], wf[2], acc[a][2]);
            }
        }
    }
    const unsigned head = 2 * pr + hh;
#pragma unroll
    for (int a = 0; a < 2; ++a) {
        const unsigned tile = tb * 4 + tp * 2 + a;
        u32x4 f0, f1;
        {
            f32x16 v = acc[a][0];
#pragma unroll
            for (int i = 0; i < 16; ++i) v[i] *= QSCALE;
            f0 = u32x4{cvtpk(v[0], v[1]), cvtpk(v[2], v[3]), cvtpk(v[4], v[5]), cvtpk(v[6], v[7])};
            f1 = u32x4{cvtpk(v[8], v[9]), cvtpk(v[10], v[11]), h ? 0u : cvtpk(v[12], v[13]), h ? 0u : cvtpk(v[14], v[15])};
            char* qp = ws + WS_Q + (size_t)(((br * 16 + head) * 64 + tile) * 2048) + lane * 16;
            *(u32x4*)qp = f0; *(u32x4*)(qp + 1024) = f1;
        }
        char* kvp = ws + WS_KV + (size_t)(((br * 16 + head) * 64 + tile) * 4096) + lane * 16;
        {
            f32x16 v = acc[a][1];
            f0 = u32x4{cvtpk(v[0], v[1]), cvtpk(v[2], v[3]), cvtpk(v[4], v[5]), cvtpk(v[6], v[7])};
            f1 = u32x4{cvtpk(v[8], v[9]), cvtpk(v[10], v[11]), h ? 0x00003f80u : cvtpk(v[12], v[13]), h ? 0u : cvtpk(v[14], v[15])};
            *(u32x4*)kvp = f0; *(u32x4*)(kvp + 1024) = f1;
        }
        {
            f32x16 v = acc[a][2];
            f0 = u32x4{cvtpk(v[0], v[1]), cvtpk(v[2], v[3]), cvtpk(v[4], v[5]), cvtpk(v[6], v[7])};
            f1 = u32x4{cvtpk(v[8], v[9]), cvtpk(v[10], v[11]), cvtpk(v[12], v[13]), cvtpk(v[14], v[15])};
            if (r >= 28) { const unsigned c = r == 28 ? 0x3f803f80u : 0u; f0 = u32x4{c, c, c, c}; f1 = f0; }
            *(u32x4*)(kvp + 2048) = f0; *(u32x4*)(kvp + 3072) = f1;
        }
    }
}

DI float rowmax16(const f32x16& s) {
    float a = __builtin_fmaxf(__builtin_fmaxf(s[0], s[1]), s[2]), b = __builtin_fmaxf(__builtin_fmaxf(s[3], s[4]), s[5]);
    a = __builtin_fmaxf(__builtin_fmaxf(a, s[6]), s[7]); b = __builtin_fmaxf(__builtin_fmaxf(b, s[8]), s[9]);
    a = __builtin_fmaxf(__builtin_fmaxf(a, s[10]), s[11]); b = __builtin_fmaxf(__builtin_fmaxf(b, s[12]), s[13]);
    a = __builtin_fmaxf(__builtin_fmaxf(a, s[14]), s[15]);
    float m = __builtin_fmaxf(a, b);
    auto rr = __builtin_amdgcn_permlane32_swap(__float_as_uint(m), __float_as_uint(m), false, false);
    return __builtin_fmaxf(__uint_as_float(rr[0]), __uint_as_float(rr[1]));
}
DI void maskit(f32x16& s, int mode, unsigned r, unsigned h) {
    if (mode == 0) return;
#pragma unroll
    for (int i = 0; i < 16; ++i) { const unsigned key = (i & 3) + 8 * (i >> 2) + 4 * h; const bool ok = mode == 1 ? key <= r : key > r; s[i] = ok ? s[i] : -INFINITY; }
}
DI void exp_pack(const f32x16& s, bf16x8& p0, bf16x8& p1) {
    float e[16];
#pragma unroll
    for (int i = 0; i < 16; ++i) e[i] = __builtin_amdgcn_exp2f(s[i]);
    p0 = __builtin_bit_cast(bf16x8, (u32x4{cvtpk(e[0], e[1]), cvtpk(e[2], e[3]), cvtpk(e[4], e[5]), cvtpk(e[6], e[7])}));
    p1 = __builtin_bit_cast(bf16x8, (u32x4{cvtpk(e[8], e[9]), cvtpk(e[10], e[11]), cvtpk(e[12], e[13]), cvtpk(e[14], e[15])}));
}
DI float set_m(bf16x8& q1, float m, unsigned h) {
    const unsigned nb = cvtpk(-m, 0.f) & 0xffffu;
    u32x4 u = __builtin_bit_cast(u32x4, q1); if (h) u[2] = nb; q1 = __builtin_bit_cast(bf16x8, u);
    return -__uint_as_float(nb << 16);
}
DI void slow_attn(const char* kv, bf16x8 q0, bf16x8 q1, int t0, int n, bool strided, int mfirst, int mlast, unsigned lane, f32x16& o, float& mout) {
    const unsigned r = lane & 31, h = lane >> 5;
    { u32x4 u = __builtin_bit_cast(u32x4, q1); if (h) u[2] = 0; q1 = __builtin_bit_cast(bf16x8, u); }
    float m = -INFINITY; o = f32x16{};
#pragma unroll 1
    for (int i = 0; i < n; ++i) {
        const int t = t0 + (strided ? 8 * (i >> 2) + (i & 3) : i);
        const char* p = kv + (size_t)t * 4096 + lane * 16;
        const bf16x8 k0 = *(const bf16x8*)p, k1 = *(const bf16x8*)(p + 1024), v0 = *(const bf16x8*)(p + 2048), v1 = *(const bf16x8*)(p + 3072);
        f32x16 s = mfma(k0, q0, f32x16{}); s = mfma(k1, q1, s);
        const int mode = (i == 0 && mfirst) ? mfirst : (i == n - 1 ? mlast : 0);
        if (mode == 1) maskit(s, 1, r, h); else if (mode == 2) maskit(s, 2, r, h);
        const float mn = __builtin_fmaxf(m, rowmax16(s));
        const float ms = mn == -INFINITY ? 0.f : mn;
        const float f = __builtin_amdgcn_exp2f(m - ms);
#pragma unroll
        for (int i = 0; i < 16; ++i) { o[i] *= f; s[i] -= ms; }
        m = mn;
        bf16x8 p0, p1; exp_pack(s, p0, p1);
        o = mfma(v0, p0, o); o = mfma(v1, p1, o);
    }
    mout = m;
}
DI bool bad16(const f32x16& o) {
    float t = 0.f;
#pragma unroll
    for (int i = 0; i < 16; ++i) t = __builtin_fmaf(o[i], 0.f, t);
    return __any(!(t == 0.f));
}
constexpr int K2_RING = 131072, K2_LDS = K2_RING + 4 * (17 * 256);
DI void combine_store(f32x16 o, float m, char* lds, unsigned qt, unsigned kh, unsigned lane, char* optr) {
    LAS float* sc = (LAS float*)(size_t)((unsigned)(size_t)lds + K2_RING + qt * (17 * 256));
    if (kh == 1) {
#pragma unroll
        for (int i = 0; i < 16; ++i) sc[i * 64 + lane] = o[i];
        sc[16 * 64 + lane] = m;
    }
    BAR();
    if (kh == 0) {
        const float m1 = sc[16 * 64 + lane];
        const float mm = __builtin_fmaxf(m, m1);
        const float f0 = m == -INFINITY ? 0.f : __builtin_amdgcn_exp2f(m - mm), f1 = m1 == -INFINITY ? 0.f : __builtin_amdgcn_exp2f(m1 - mm);
#pragma unroll
        for (int i = 0; i < 16; ++i) o[i] = o[i] * f0 + sc[i * 64 + lane] * f1;
        auto rr = __builtin_amdgcn_permlane32_swap(__float_as_uint(o[12]), __float_as_uint(o[12]), false, false);
        const float l = __uint_as_float(rr[1]);
        const float inv = 1.f / l;
        const unsigned h = lane >> 5;
        u32x4 f0v = {cvtpk(o[0] * inv, o[1] * inv), cvtpk(o[2] * inv, o[3] * inv), cvtpk(o[4] * inv, o[5] * inv), cvtpk(o[6] * inv, o[7] * inv)};
        u32x4 f1v = {cvtpk(o[8] * inv, o[9] * inv), cvtpk(o[10] * inv, o[11] * inv), h ? 0u : cvtpk(o[12] * inv, o[13] * inv), h ? 0u : cvtpk(o[14] * inv, o[15] * inv)};
        *(u32x4*)optr = f0v; *(u32x4*)(optr + 1024) = f1v;
    }
}

__global__ void __launch_bounds__(512, 2) k_attn(char* __restrict__ ws) {
    extern __shared__ __attribute__((aligned(16))) char lds[];
    const unsigned tid = threadIdx.x, lane = tid & 63, wid = rfl(tid >> 6), r = lane & 31, h = lane >> 5;
    const unsigned bid = blockIdx.x, head = 2 * (bid & 7) + ((bid >> 3) >> 4), qb = (bid >> 3) & 15;
    const unsigned qt = wid & 3, kh = wid >> 2, T = qb * 4 + qt;
    const unsigned lds0 = (unsigned)(size_t)lds;
    const char* kvA = ws + WS_KV + (size_t)(0 * 16 + head) * 64 * 4096;
    const char* kvB = ws + WS_KV + (size_t)(1 * 16 + head) * 64 * 4096;
    const __amdgpu_buffer_rsrc_t rA = mk_rsrc(kvA, 64 * 4096), rB = mk_rsrc(kvB, 64 * 4096);
    const unsigned vl = lane * 16;
    const int a0 = (int)qb * 4 - 8;
    auto issue = [&](int s) {
        const unsigned slot = lds0 + (s & 3) * 32768 + wid * 4096;
#pragma unroll
        for (int i = 0; i < 4; ++i) {
            if (s < 8) dma16(slot + i * 1024, vl, rB, s * 32768 + wid * 4096 + i * 1024);
            else { const int tile = a0 + (s - 8) * 8 + (int)wid;
                   const unsigned so = (tile < 0 || tile >= 64) ? 0x80000000u : (unsigned)tile * 4096 + i * 1024;
                   dma16(slot + i * 1024, vl, rA, so); }
        }
    };
    bf16x8 qB0, qB1, qA0, qA1;
    {
        const char* qpB = ws + WS_Q + (size_t)((16 + head) * 64 + T) * 2048 + lane * 16;
        const char* qpA = ws + WS_Q + (size_t)((0 + head) * 64 + T) * 2048 + lane * 16;
        asm volatile("global_load_dwordx4 %0, %4, off\n\tglobal_load_dwordx4 %1, %4, off offset:1024\n\tglobal_load_dwordx4 %2, %5, off\n\tglobal_load_dwordx4 %3, %5, off offset:1024"
                     : "=&v"(qB0), "=&v"(qB1), "=&v"(qA0), "=&v"(qA1) : "v"(qpB), "v"(qpA) : "memory");
    }
    issue(0); issue(1); issue(2);
    asm volatile("s_waitcnt vmcnt(8)" : "+v"(qB0), "+v"(qB1), "+v"(qA0), "+v"(qA1) :: "memory");
    BAR();
    f32x16 o = f32x16{}; float m;
    const unsigned kb = lds0 + kh * 16384 + vl;
    {
        f32x16 s = mfma(ldsr128(kb), qB0, f32x16{}); s = mfma(ldsr128(kb + 1024), qB1, s);
        m = set_m(qB1, rowmax16(s) + MBIAS, h);
    }
    f32x16 sc = mfma(ldsr128(kb), qB0, f32x16{}); sc = mfma(ldsr128(kb + 1024), qB1, sc);
#pragma unroll 1
    for (int s = 0; s < 8; ++s) {
        const unsigned cur = kb + (s & 3) * 32768, nxt = kb + ((s + 1) & 3) * 32768;
#pragma unroll
        for (int j = 0; j < 4; ++j) {
            if (j == 3) {
                if (s < 7) WAITV(4); else WAITV(4);
                BAR();
                if (s + 3 < 10) issue(s + 3);
            }
            const unsigned kn = j < 3 ? cur + (j + 1) * 4096 : nxt, vc = cur + j * 4096 + 2048;
            const bf16x8 k0 = ldsr128(kn), k1 = ldsr128(kn + 1024), v0 = ldsr128(vc), v1 = ldsr128(vc + 1024);
            bf16x8 p0, p1; exp_pack(sc, p0, p1);
            f32x16 sn = mfma(k0, qB0, f32x16{}); sn = mfma(k1, qB1, sn);
            o = mfma(v0, p0, o); o = mfma(v1, p1, o);
            sc = sn;
        }
    }
    if (bad16(o)) slow_attn(kvB, qB0, qB1, kh * 4, 32, true, 0, 0, lane, o, m);
    combine_store(o, m, lds, qt, kh, lane, ws + WS_O + (size_t)(((64 + T) * 16 + head) * 2048) + lane * 16);
    WAITV(0); BAR();
    {
        const int tlo = kh ? ((int)T - 3 < 0 ? 0 : (int)T - 3) : ((int)T - 8 < 0 ? 0 : (int)T - 8);
        const int thi = kh ? (int)T : (int)T - 4;
        const int mfirst = (!kh && (int)T >= 8) ? 2 : 0, mlast = kh ? 1 : 0;
        f32x16 oa = f32x16{}; float ma = -INFINITY;
        if (thi >= tlo) {
            const unsigned base = lds0 + vl - (unsigned)a0 * 4096;
            {
                const int tp = kh ? thi : (mfirst ? tlo + 1 : tlo);
                f32x16 s = mfma(ldsr128(base + tp * 4096), qA0, f32x16{}); s = mfma(ldsr128(base + tp * 4096 + 1024), qA1, s);
                if (kh) maskit(s, 1, r, h);
                ma = set_m(qA1, rowmax16(s) + MBIAS, h);
            }
#pragma unroll 1
            for (int t = tlo; t <= thi; ++t) {
                const unsigned a = base + t * 4096;
                f32x16 s = mfma(ldsr128(a), qA0, f32x16{}); s = mfma(ldsr128(a + 1024), qA1, s);
                if (t == tlo && mfirst) maskit(s, 2, r, h);
                if (t == thi && mlast) maskit(s, 1, r, h);
                bf16x8 p0, p1; exp_pack(s, p0, p1);
                oa = mfma(ldsr128(a + 2048), p0, oa); oa = mfma(ldsr128(a + 3072), p1, oa);
            }
            if (bad16(oa)) slow_attn(kvA, qA0, qA1, tlo, thi - tlo + 1, false, mfirst, mlast, lane, oa, ma);
        }
        BAR();
        combine_store(oa, ma, lds, qt, kh, lane, ws + WS_O + (size_t)(((0 + T) * 16 + head) * 2048) + lane * 16);
    }
}

constexpr int K3_LDS = 57 * 1024;
__global__ void __launch_bounds__(256, 1) k_out(const uint16_t* __restrict__ woA, const uint16_t* __restrict__ woB, const char* __restrict__ ws, uint16_t* __restrict__ out) {
    extern __shared__ __attribute__((aligned(16))) char lds[];
    const unsigned tid = threadIdx.x, lane = tid & 63, wid = rfl(tid >> 6), r = lane & 31, h = lane >> 5;
    const unsigned bid = blockIdx.x;
    const unsigned g = bid & 7, j = bid >> 3;
    const unsigned br = g & 1, tb = (g >> 1) * 4 + (j & 3), cg = j >> 2;
    const __amdgpu_buffer_rsrc_t rw = mk_rsrc(br ? woB : woA, D_ * D_ * 2);
    const unsigned lds0 = (unsigned)(size_t)lds;
    const unsigned vw = (lane >> 3) * 896 + (((lane & 7) ^ (((lane >> 4) & 1) << 2)) << 4);
    const unsigned tile = tb * 4 + wid;
    const char* op = ws + WS_O + (size_t)((br * 64 + tile) * 16) * 2048 + lane * 16;
    for (unsigned i = wid; i < 57; i += 4) dma16(lds0 + i * 1024, vw, rw, i < 56 ? i * 8 * 896 + cg * 128 : 0x80000000u);
    bf16x8 of[32];
#pragma unroll
    for (int i = 0; i < 32; ++i) of[i] = *(const bf16x8*)(op + i * 1024);
    WAITV(0); BAR();
    const unsigned q4 = (lane & 15) >> 2, p4 = lane & 3, blk = (lane >> 4) & 1;
    unsigned wa[2];
#pragma unroll
    for (int nt = 0; nt < 2; ++nt) { const unsigned lb = 64 * nt + 32 * blk + 8 * p4;
        wa[nt] = lds0 + (4 * h + q4) * 128 + ((((lb >> 4) ^ (((q4 >> 1) & 1) << 2)) << 4) | (lb & 15)); }
    f32x16 acc[2] = {f32x16{}, f32x16{}};
#pragma unroll
    for (int hd = 0; hd < 16; ++hd)
#pragma unroll
        for (int s = 0; s < 2; ++s)
#pragma unroll
            for (int nt = 0; nt < 2; ++nt) {
                const unsigned a = wa[nt] + (hd * 28 + 16 * s) * 128;
                acc[nt] = mfma(cat8(ldstr(a), ldstr(a + 8 * 128)), of[hd * 2 + s], acc[nt]);
            }
    uint16_t* orow = out + (size_t)(tile * 32 + r) * 896 + br * 448 + cg * 64 + 8 * h;
#pragma unroll
    for (int nt = 0; nt < 2; ++nt)
#pragma unroll
        for (int k = 0; k < 2; ++k) {
            const f32x16 v = acc[nt];
            unsigned a0 = cvtpk(v[8 * k], v[8 * k + 1]), a1 = cvtpk(v[8 * k + 2], v[8 * k + 3]), b0 = cvtpk(v[8 * k + 4], v[8 * k + 5]), b1 = cvtpk(v[8 * k + 6], v[8 * k + 7]);
            auto r0 = __builtin_amdgcn_permlane32_swap(a0, b0, false, false);
            auto r1 = __builtin_amdgcn_permlane32_swap(a1, b1, false, false);
            *(u32x4*)(orow + nt * 32 + 16 * k) = u32x4{r0[0], r1[0], r0[1], r1[1]};
        }
}

extern "C" void kernel_launch(void* const* d_in, const int* in_sizes, int n_in, void* d_out, int out_size, void* d_ws, size_t ws_size, hipStream_t stream) {
    const uint16_t* xA = (const uint16_t*)d_in[0]; const uint16_t* wA = (const uint16_t*)d_in[1]; const uint16_t* woA = (const uint16_t*)d_in[2];
    const uint16_t* xB = (const uint16_t*)d_in[3]; const uint16_t* wB = (const uint16_t*)d_in[4]; const uint16_t* woB = (const uint16_t*)d_in[5];
    (void)hipFuncSetAttribute((const void*)k_qkv, hipFuncAttributeMaxDynamicSharedMemorySize, K1_LDS);
    (void)hipFuncSetAttribute((const void*)k_attn, hipFuncAttributeMaxDynamicSharedMemorySize, K2_LDS);
    (void)hipFuncSetAttribute((const void*)k_out, hipFuncAttributeMaxDynamicSharedMemorySize, K3_LDS);
    k_qkv<<<256, 256, K1_LDS, stream>>>(xA, wA, xB, wB, (char*)d_ws);
    k_attn<<<256, 512, K2_LDS, stream>>>((char*)d_ws);
    k_out<<<224, 256, K3_LDS, stream>>>(woA, woB, (const char*)d_ws, (uint16_t*)d_out);
}
```

```cpp
#include <hip/hip_runtime.h>
#include <hip/hip_bf16.h>
#include <stdint.h>

typedef __bf16 bf16x8 __attribute__((ext_vector_type(8)));
typedef __bf16 bf16x2 __attribute__((ext_vector_type(2)));
typedef float f32x16 __attribute__((ext_vector_type(16)));
typedef float f32x4 __attribute__((ext_vector_type(4)));
typedef float f32x2 __attribute__((ext_vector_type(2)));
typedef unsigned u32x4 __attribute__((ext_vector_type(4)));
typedef unsigned u32x2 __attribute__((ext_vector_type(2)));
typedef short s16x4 __attribute__((ext_vector_type(4)));
typedef short s16x8 __attribute__((ext_vector_type(8)));
typedef unsigned long long u64;
#define LAS __attribute__((address_space(3)))
#define GAS __attribute__((address_space(1)))
#define DI __device__ __forceinline__

constexpr int S_ = 2048, D_ = 448;
constexpr unsigned WS_KV = 0;
constexpr unsigned WS_O = 8u << 20;
constexpr unsigned WS_Q = 12u << 20;
constexpr unsigned WS_E = 16u << 20;
constexpr unsigned WS_FKV = WS_E + 4096;
constexpr unsigned WS_FO = WS_FKV + 65536;
constexpr float QSCALE = 0.18898223650461362f * 1.4426950408889634f;
#define MBIASV 100
constexpr float MBIAS = (float)MBIASV;
constexpr unsigned P1_SLOT = 28672;
constexpr unsigned L_PARK = 65536;
constexpr unsigned L_AKV = 98304;
constexpr unsigned L_Q = 147456;
constexpr unsigned L_SYNC = 155648;
constexpr unsigned L_PM = 156160;
constexpr unsigned L_SCR = 65536;
constexpr unsigned L_HSCR = 81920;
constexpr unsigned L_WO = 98304;
constexpr unsigned LDS_BYTES = 163840;
constexpr unsigned SY_CMB = 0;
constexpr unsigned SY_RD = 32;
constexpr unsigned SY_RED = 64;
constexpr unsigned SY_ARD = 96;
constexpr unsigned SY_LND = 128, SY_FREE = 160;
constexpr unsigned SY_AST = 124;
constexpr unsigned SY_DUMMY = 196;
constexpr unsigned SY_ADN = 192;
constexpr unsigned SY_AL = 112;
constexpr unsigned SY_W1 = 100, SY_W1F = 104, SY_W2 = 108;

DI f32x16 mfma(bf16x8 a, bf16x8 b, f32x16 c) { return __builtin_amdgcn_mfma_f32_32x32x16_bf16(a, b, c, 0, 0, 0); }
DI unsigned cvtpk(float lo, float hi) { f32x2 v = {lo, hi}; bf16x2 b = __builtin_convertvector(v, bf16x2); return __builtin_bit_cast(unsigned, b); }
DI unsigned rfl(unsigned x) { return __builtin_amdgcn_readfirstlane(x); }
DI __amdgpu_buffer_rsrc_t mk_rsrc(const void* p, unsigned bytes) { return __builtin_amdgcn_make_buffer_rsrc((void*)p, (short)0, (int)bytes, 0x00020000); }
DI void dma16(unsigned lds_dst, unsigned voff, __amdgpu_buffer_rsrc_t rsrc, unsigned soff) {
    asm volatile("s_mov_b32 m0, %0\n\ts_nop 0\n\tbuffer_load_dwordx4 %1, %2, %3 offen lds" :: "s"(lds_dst), "v"(voff), "s"(rsrc), "s"(soff) : "memory");
}
DI void dma16c(unsigned lds_dst, unsigned voff, __amdgpu_buffer_rsrc_t rsrc, unsigned soff) {
    asm volatile("s_mov_b32 m0, %0\n\ts_nop 0\n\tbuffer_load_dwordx4 %1, %2, %3 offen sc1 lds" :: "s"(lds_dst), "v"(voff), "s"(rsrc), "s"(soff) : "memory");
}
DI bf16x8 ldsr128(unsigned addr) { return *(const LAS bf16x8*)(size_t)addr; }
DI f32x4 ldsr128f(unsigned addr) { return *(const LAS f32x4*)(size_t)addr; }
DI void ldsw128(unsigned addr, u32x4 v) { *(LAS u32x4*)(size_t)addr = v; }
typedef short v4i16_t __attribute__((ext_vector_type(4)));
DI s16x4 ldstr(unsigned addr) { return __builtin_amdgcn_ds_read_tr16_b64_v4i16((LAS v4i16_t*)(size_t)addr); }
DI bf16x8 cat8(s16x4 a, s16x4 b) { s16x8 r = {a[0], a[1], a[2], a[3], b[0], b[1], b[2], b[3]}; return __builtin_bit_cast(bf16x8, r); }
DI void st128c(u32x4 v, __amdgpu_buffer_rsrc_t rs, unsigned voff, unsigned soff) { __builtin_amdgcn_raw_buffer_store_b128(v, rs, voff, soff, 16); }
DI u32x4 ld128c(__amdgpu_buffer_rsrc_t rs, unsigned voff, unsigned soff) { return __builtin_amdgcn_raw_buffer_load_b128(rs, voff, soff, 16); }
#define STAMP_DECL
#define STAMP(i)
#define STAMPN(i)
#define STAMP_FLUSH()
#define STAMP_ARG
#define STAMP_PASS
#define WAITV(n) asm volatile("s_waitcnt vmcnt(" #n ")" ::: "memory")
#define BAR() asm volatile("s_waitcnt lgkmcnt(0)\n\ts_barrier" ::: "memory")
constexpr unsigned SPIN_CAP = 1u << 16;
DI unsigned lds_ld(unsigned addr) { return __hip_atomic_load((LAS unsigned*)(size_t)addr, __ATOMIC_RELAXED, __HIP_MEMORY_SCOPE_WORKGROUP); }
DI void lds_st(unsigned addr, unsigned v) { __hip_atomic_store((LAS unsigned*)(size_t)addr, v, __ATOMIC_RELAXED, __HIP_MEMORY_SCOPE_WORKGROUP); }
DI void lds_signal(unsigned addr, unsigned v, unsigned lane) { asm volatile("s_waitcnt lgkmcnt(0)" ::: "memory"); if (lane == 0) lds_st(addr, v); asm volatile("" ::: "memory"); }
DI void lds_arrive(unsigned addr, unsigned) {
    asm volatile("s_waitcnt lgkmcnt(0)\n\ts_mov_b64 exec, 1\n\tds_add_u32 %0, %1\n\ts_mov_b64 exec, -1" :: "v"(addr), "v"(1u) : "memory");
}
template <int SLEEP> DI void lds_wait(unsigned addr, unsigned n) {
    unsigned v, s, cap;
    asm volatile("s_mov_b32 %2, 0x10000\n"
                 "1:\n\tds_read_b32 %0, %3\n\ts_waitcnt lgkmcnt(0)\n\tv_readfirstlane_b32 %1, %0\n\ts_sub_u32 %2, %2, 1\n\ts_cbranch_scc1 2f\n\t"
                 "s_cmp_ge_u32 %1, %4\n\ts_cbranch_scc1 2f\n\ts_sleep %5\n\ts_branch 1b\n2:"
                 : "=&v"(v), "=&s"(s), "=&s"(cap) : "v"(addr), "s"(n), "i"(SLEEP) : "scc", "memory");
}
DI void lds_wait_ge(unsigned addr, unsigned n) { lds_wait<0>(addr, n); }
DI void lds_wait_ge_sleep(unsigned addr, unsigned n) { lds_wait<4>(addr, n); }
DI void poll_flags(const char* flag, u64 tag) {
    const GAS u64* p = (const GAS u64*)flag;
    for (unsigned spins = 0; spins < SPIN_CAP; ++spins) {
        const u64 v = __hip_atomic_load(p, __ATOMIC_RELAXED, __HIP_MEMORY_SCOPE_AGENT);
        if (__all(v == tag)) break;
        __builtin_amdgcn_s_sleep(2);
    }
    asm volatile("" ::: "memory");
}
DI bool try_flags(const char* flag, u64 tag) {
    const GAS u64* p = (const GAS u64*)flag;
    const u64 v = __hip_atomic_load(p, __ATOMIC_RELAXED, __HIP_MEMORY_SCOPE_AGENT);
    const bool ok = __all(v == tag);
    asm volatile("" ::: "memory");
    return ok;
}
DI void set_flag(char* p, u64 tag) { __hip_atomic_store((GAS u64*)p, tag, __ATOMIC_RELAXED, __HIP_MEMORY_SCOPE_AGENT); }
DI u64 mix64(u64 z) { z += 0x9E3779B97F4A7C15ull; z = (z ^ (z >> 30)) * 0xBF58476D1CE4E5B9ull; z = (z ^ (z >> 27)) * 0x94D049BB133111EBull; return z ^ (z >> 31); }
DI u64 rfl64(u64 v) { return ((u64)rfl((unsigned)(v >> 32)) << 32) | rfl((unsigned)v); }

DI float rowmax16(const f32x16& s) {
    float a = __builtin_fmaxf(__builtin_fmaxf(s[0], s[1]), s[2]), b = __builtin_fmaxf(__builtin_fmaxf(s[3], s[4]), s[5]);
    a = __builtin_fmaxf(__builtin_fmaxf(a, s[6]), s[7]); b = __builtin_fmaxf(__builtin_fmaxf(b, s[8]), s[9]);
    a = __builtin_fmaxf(__builtin_fmaxf(a, s[10]), s[11]); b = __builtin_fmaxf(__builtin_fmaxf(b, s[12]), s[13]);
    a = __builtin_fmaxf(__builtin_fmaxf(a, s[14]), s[15]);
    return __builtin_fmaxf(a, b);
}
DI float xhalf_max(float m) {
    auto rr = __builtin_amdgcn_permlane32_swap(__float_as_uint(m), __float_as_uint(m), false, false);
    return __builtin_fmaxf(__uint_as_float(rr[0]), __uint_as_float(rr[1]));
}
DI void maskit(f32x16& s, int mode, unsigned r, unsigned h) {
#pragma unroll
    for (int i = 0; i < 16; ++i) { const unsigned key = (i & 3) + 8 * (i >> 2) + 4 * h; const bool ok = mode == 1 ? key <= r : key > r; s[i] = ok ? s[i] : -INFINITY; }
}
DI void exp_pack(const f32x16& s, bf16x8& p0, bf16x8& p1) {
    float e[16];
#pragma unroll
    for (int i = 0; i < 16; ++i) e[i] = __builtin_amdgcn_exp2f(s[i]);
    p0 = __builtin_bit_cast(bf16x8, (u32x4{cvtpk(e[0], e[1]), cvtpk(e[2], e[3]), cvtpk(e[4], e[5]), cvtpk(e[6], e[7])}));
    p1 = __builtin_bit_cast(bf16x8, (u32x4{cvtpk(e[8], e[9]), cvtpk(e[10], e[11]), cvtpk(e[12], e[13]), cvtpk(e[14], e[15])}));
}
DI float set_m(bf16x8& q1, float m, unsigned h) {
    const unsigned nb = cvtpk(-m, 0.f) & 0xffffu;
    u32x4 u = __builtin_bit_cast(u32x4, q1); if (h) u[2] = nb; q1 = __builtin_bit_cast(bf16x8, u);
    return -__uint_as_float(nb << 16);
}
DI f32x16 qk(bf16x8 k0, bf16x8 k1, bf16x8 q0, bf16x8 q1) { f32x16 s = mfma(k0, q0, f32x16{}); return mfma(k1, q1, s); }
DI void slow_attn(__amdgpu_buffer_rsrc_t rkv, unsigned headoff, bf16x8 q0, bf16x8 q1, int t0, int n, bool strided, int mfirst, int mlast, unsigned lane, f32x16& o, float& mout) {
    const unsigned r = lane & 31, h = lane >> 5;
    { u32x4 u = __builtin_bit_cast(u32x4, q1); if (h) u[2] = 0; q1 = __builtin_bit_cast(bf16x8, u); }
    float m = -INFINITY; o = f32x16{};
#pragma unroll 1
    for (int i = 0; i < n; ++i) {
        const int t = t0 + (strided ? 8 * (i >> 2) + (i & 3) : i);
        const unsigned so = headoff + (unsigned)t * 4096;
        const bf16x8 k0 = __builtin_bit_cast(bf16x8, ld128c(rkv, lane * 16, so)), k1 = __builtin_bit_cast(bf16x8, ld128c(rkv, lane * 16, so + 1024));
        const bf16x8 v0 = __builtin_bit_cast(bf16x8, ld128c(rkv, lane * 16, so + 2048)), v1 = __builtin_bit_cast(bf16x8, ld128c(rkv, lane * 16, so + 3072));
        f32x16 s = qk(k0, k1, q0, q1);
        if (i == 0 && mfirst) maskit(s, 2, r, h);
        if (i == n - 1 && mlast) maskit(s, 1, r, h);
        const float mn = __builtin_fmaxf(m, xhalf_max(rowmax16(s)));
        const float ms = mn == -INFINITY ? 0.f : mn;
        const float f = __builtin_amdgcn_exp2f(m - ms);
#pragma unroll
        for (int k = 0; k < 16; ++k) { o[k] *= f; s[k] -= ms; }
        m = mn;
        bf16x8 p0, p1; exp_pack(s, p0, p1);
        o = mfma(v0, p0, o); o = mfma(v1, p1, o);
    }
    mout = m;
}
#define PARK_X \
    "ds_write_b128 %[pa], v[32:35] offset:0\n\t" \
    "ds_write_b128 %[pa], v[36:39] offset:1024\n\t" \
    "ds_write_b128 %[pa], v[40:43] offset:2048\n\t" \
    "ds_write_b128 %[pa], v[44:47] offset:3072\n\t" \
    "ds_write_b32 %[pm], %[ms]\n\t" \
    "v_mov_b32 v0, 1\n\t" \
    "s_waitcnt lgkmcnt(0)\n\t" \
    "s_mov_b64 exec, 1\n\t" \
    "ds_write_b32 %[cm], v0\n\t" \
    "s_mov_b64 exec, -1\n\t" \
    "s_mov_b32 %[cap], 0x10000\n\t" \
    "1:\n\t" \
    "ds_read_b32 v1, %[cp]\n\t" \
    "s_waitcnt lgkmcnt(0)\n\t" \
    "v_readfirstlane_b32 %[t0], v1\n\t" \
    "s_sub_u32 %[cap], %[cap], 1\n\t" \
    "s_cbranch_scc1 2f\n\t" \
    "s_cmp_eq_u32 %[t0], 0\n\t" \
    "s_cbranch_scc1 1b\n\t" \
    "2:\n\t" \
    ""
#define PARK_Y \
    "ds_write_b128 %[pa], v[128:131] offset:0\n\t" \
    "ds_write_b128 %[pa], v[132:135] offset:1024\n\t" \
    "ds_write_b128 %[pa], v[136:139] offset:2048\n\t" \
    "ds_write_b128 %[pa], v[140:143] offset:3072\n\t" \
    "ds_write_b32 %[pm], %[ms]\n\t" \
    "v_mov_b32 v0, 1\n\t" \
    "s_waitcnt lgkmcnt(0)\n\t" \
    "s_mov_b64 exec, 1\n\t" \
    "ds_write_b32 %[cm], v0\n\t" \
    "s_mov_b64 exec, -1\n\t" \
    "s_mov_b32 %[cap], 0x10000\n\t" \
    "1:\n\t" \
    "ds_read_b32 v1, %[cp]\n\t" \
    "s_waitcnt lgkmcnt(0)\n\t" \
    "v_readfirstlane_b32 %[t0], v1\n\t" \
    "s_sub_u32 %[cap], %[cap], 1\n\t" \
    "s_cbranch_scc1 2f\n\t" \
    "s_cmp_eq_u32 %[t0], 0\n\t" \
    "s_cbranch_scc1 1b\n\t" \
    "2:\n\t" \
    ""
#define OUTPROJ_WO \
    "ds_read_b64_tr_b16 v[64:65], %[wa0] offset:0\n\t" \
    "ds_read_b64_tr_b16 v[66:67], %[wa0] offset:1024\n\t" \
    "ds_read_b64_tr_b16 v[72:73], %[wa0] offset:2048\n\t" \
    "ds_read_b64_tr_b16 v[74:75], %[wa0] offset:3072\n\t" \
    "ds_read_b64_tr_b16 v[80:81], %[wa0] offset:3584\n\t" \
    "ds_read_b64_tr_b16 v[82:83], %[wa0] offset:4608\n\t" \
    "ds_read_b64_tr_b16 v[88:89], %[wa0] offset:5632\n\t" \
    "ds_read_b64_tr_b16 v[90:91], %[wa0] offset:6656\n\t" \
    "ds_read_b64_tr_b16 v[96:97], %[wa0] offset:7168\n\t" \
    "ds_read_b64_tr_b16 v[98:99], %[wa0] offset:8192\n\t" \
    "ds_read_b64_tr_b16 v[104:105], %[wa0] offset:9216\n\t" \
    "ds_read_b64_tr_b16 v[106:107], %[wa0] offset:10240\n\t" \
    "ds_read_b64_tr_b16 v[112:113], %[wa0] offset:10752\n\t" \
    "ds_read_b64_tr_b16 v[114:115], %[wa0] offset:11776\n\t" \
    "ds_read_b64_tr_b16 v[120:121], %[wa0] offset:12800\n\t" \
    "ds_read_b64_tr_b16 v[122:123], %[wa0] offset:13824\n\t" \
    "ds_read_b64_tr_b16 v[128:129], %[wa0] offset:14336\n\t" \
    "ds_read_b64_tr_b16 v[130:131], %[wa0] offset:15360\n\t" \
    "ds_read_b64_tr_b16 v[136:137], %[wa0] offset:16384\n\t" \
    "ds_read_b64_tr_b16 v[138:139], %[wa0] offset:17408\n\t" \
    "ds_read_b64_tr_b16 v[144:145], %[wa0] offset:17920\n\t" \
    "ds_read_b64_tr_b16 v[146:147], %[wa0] offset:18944\n\t" \
    "ds_read_b64_tr_b16 v[152:153], %[wa0] offset:19968\n\t" \
    "ds_read_b64_tr_b16 v[154:155], %[wa0] offset:20992\n\t" \
    "ds_read_b64_tr_b16 v[160:161], %[wa0] offset:21504\n\t" \
    "ds_read_b64_tr_b16 v[162:163], %[wa0] offset:22528\n\t" \
    "ds_read_b64_tr_b16 v[168:169], %[wa0] offset:23552\n\t" \
    "ds_read_b64_tr_b16 v[170:171], %[wa0] offset:24576\n\t" \
    "ds_read_b64_tr_b16 v[176:177], %[wa0] offset:25088\n\t" \
    "ds_read_b64_tr_b16 v[178:179], %[wa0] offset:26112\n\t" \
    "ds_read_b64_tr_b16 v[184:185], %[wa0] offset:27136\n\t" \
    "ds_read_b64_tr_b16 v[186:187], %[wa0] offset:28160\n\t" \
    "ds_read_b64_tr_b16 v[68:69], %[wa1] offset:0\n\t" \
    "ds_read_b64_tr_b16 v[70:71], %[wa1] offset:1024\n\t" \
    "ds_read_b64_tr_b16 v[76:77], %[wa1] offset:2048\n\t" \
    "ds_read_b64_tr_b16 v[78:79], %[wa1] offset:3072\n\t" \
    "ds_read_b64_tr_b16 v[84:85], %[wa1] offset:3584\n\t" \
    "ds_read_b64_tr_b16 v[86:87], %[wa1] offset:4608\n\t" \
    "ds_read_b64_tr_b16 v[92:93], %[wa1] offset:5632\n\t" \
    "ds_read_b64_tr_b16 v[94:95], %[wa1] offset:6656\n\t" \
    "ds_read_b64_tr_b16 v[100:101], %[wa1] offset:7168\n\t" \
    "ds_read_b64_tr_b16 v[102:103], %[wa1] offset:8192\n\t" \
    "ds_read_b64_tr_b16 v[108:109], %[wa1] offset:9216\n\t" \
    "ds_read_b64_tr_b16 v[110:111], %[wa1] offset:10240\n\t" \
    "ds_read_b64_tr_b16 v[116:117], %[wa1] offset:10752\n\t" \
    "ds_read_b64_tr_b16 v[118:119], %[wa1] offset:11776\n\t" \
    "ds_read_b64_tr_b16 v[124:125], %[wa1] offset:12800\n\t" \
    "ds_read_b64_tr_b16 v[126:127], %[wa1] offset:13824\n\t" \
    "ds_read_b64_tr_b16 v[132:133], %[wa1] offset:14336\n\t" \
    "ds_read_b64_tr_b16 v[134:135], %[wa1] offset:15360\n\t" \
    "ds_read_b64_tr_b16 v[140:141], %[wa1] offset:16384\n\t" \
    "ds_read_b64_tr_b16 v[142:143], %[wa1] offset:17408\n\t" \
    "ds_read_b64_tr_b16 v[148:149], %[wa1] offset:17920\n\t" \
    "ds_read_b64_tr_b16 v[150:151], %[wa1] offset:18944\n\t" \
    "ds_read_b64_tr_b16 v[156:157], %[wa1] offset:19968\n\t" \
    "ds_read_b64_tr_b16 v[158:159], %[wa1] offset:20992\n\t" \
    "ds_read_b64_tr_b16 v[164:165], %[wa1] offset:21504\n\t" \
    "ds_read_b64_tr_b16 v[166:167], %[wa1] offset:22528\n\t" \
    "ds_read_b64_tr_b16 v[172:173], %[wa1] offset:23552\n\t" \
    "ds_read_b64_tr_b16 v[174:175], %[wa1] offset:24576\n\t" \
    "ds_read_b64_tr_b16 v[180:181], %[wa1] offset:25088\n\t" \
    "ds_read_b64_tr_b16 v[182:183], %[wa1] offset:26112\n\t" \
    "ds_read_b64_tr_b16 v[188:189], %[wa1] offset:27136\n\t" \
    "ds_read_b64_tr_b16 v[190:191], %[wa1] offset:28160\n\t" \
    "s_waitcnt lgkmcnt(0)\n\t" \
    ""
#define OUTPROJ_RUN \
    "global_load_dwordx2 v[224:225], %[fl], off sc1\n\t" \
    "v_mov_b64 v[192:193], 0\n\t" \
    "v_mov_b64 v[194:195], 0\n\t" \
    "v_mov_b64 v[196:197], 0\n\t" \
    "v_mov_b64 v[198:199], 0\n\t" \
    "v_mov_b64 v[200:201], 0\n\t" \
    "v_mov_b64 v[202:203], 0\n\t" \
    "v_mov_b64 v[204:205], 0\n\t" \
    "v_mov_b64 v[206:207], 0\n\t" \
    "v_mov_b64 v[208:209], 0\n\t" \
    "v_mov_b64 v[210:211], 0\n\t" \
    "v_mov_b64 v[212:213], 0\n\t" \
    "v_mov_b64 v[214:215], 0\n\t" \
    "v_mov_b64 v[216:217], 0\n\t" \
    "v_mov_b64 v[218:219], 0\n\t" \
    "v_mov_b64 v[220:221], 0\n\t" \
    "v_mov_b64 v[222:223], 0\n\t" \
    "s_mov_b32 %[iss], 0\n\t" \
    "s_mov_b32 %[nxt], 0\n\t" \
    "s_mov_b32 %[lnd], 0\n\t" \
    "s_mov_b32 %[pend], 0\n\t" \
    "s_waitcnt vmcnt(0)\n\t" \
    "v_mov_b32 v226, 1\n\t" \
    "s_mov_b64 exec, 1\n\t" \
    "ds_add_u32 %[arr], v226\n\t" \
    "s_mov_b64 exec, -1\n\t" \
    "v_cmp_eq_u64 vcc, %[tag], v[224:225]\n\t" \
    "s_nop 1\n\t" \
    "s_and_b32 %[t0], vcc_lo, 0xff\n\t" \
    "s_mov_b32 %[pend], %[t0]\n\t" \
    "s_cmp_eq_u32 %[t0], 0xff\n\t" \
    "s_cbranch_scc0 60f\n\t" \
    "s_add_u32 %[t0], %[so], 0\n\t" \
    "buffer_load_dwordx4 v[0:3], %[vl], %[srd], %[t0] offen sc1\n\t" \
    "buffer_load_dwordx4 v[4:7], %[vl], %[srd], %[t0] offen offset:1024 sc1\n\t" \
    "s_add_u32 %[t0], %[so], 2048\n\t" \
    "buffer_load_dwordx4 v[8:11], %[vl], %[srd], %[t0] offen sc1\n\t" \
    "buffer_load_dwordx4 v[12:15], %[vl], %[srd], %[t0] offen offset:1024 sc1\n\t" \
    "s_add_u32 %[t0], %[so], 4096\n\t" \
    "buffer_load_dwordx4 v[16:19], %[vl], %[srd], %[t0] offen sc1\n\t" \
    "buffer_load_dwordx4 v[20:23], %[vl], %[srd], %[t0] offen offset:1024 sc1\n\t" \
    "s_add_u32 %[t0], %[so], 6144\n\t" \
    "buffer_load_dwordx4 v[24:27], %[vl], %[srd], %[t0] offen sc1\n\t" \
    "buffer_load_dwordx4 v[28:31], %[vl], %[srd], %[t0] offen offset:1024 sc1\n\t" \
    "s_add_u32 %[t0], %[so], 8192\n\t" \
    "buffer_load_dwordx4 v[32:35], %[vl], %[srd], %[t0] offen sc1\n\t" \
    "buffer_load_dwordx4 v[36:39], %[vl], %[srd], %[t0] offen offset:1024 sc1\n\t" \
    "s_add_u32 %[t0], %[so], 10240\n\t" \
    "buffer_load_dwordx4 v[40:43], %[vl], %[srd], %[t0] offen sc1\n\t" \
    "buffer_load_dwordx4 v[44:47], %[vl], %[srd], %[t0] offen offset:1024 sc1\n\t" \
    "s_add_u32 %[t0], %[so], 12288\n\t" \
    "buffer_load_dwordx4 v[48:51], %[vl], %[srd], %[t0] offen sc1\n\t" \
    "buffer_load_dwordx4 v[52:55], %[vl], %[srd], %[t0] offen offset:1024 sc1\n\t" \
    "s_add_u32 %[t0], %[so], 14336\n\t" \
    "buffer_load_dwordx4 v[56:59], %[vl], %[srd], %[t0] offen sc1\n\t" \
    "buffer_load_dwordx4 v[60:63], %[vl], %[srd], %[t0] offen offset:1024 sc1\n\t" \
    "s_waitcnt vmcnt(14)\n\t" \
    "v_mfma_f32_32x32x16_bf16 v[192:207], v[64:67], v[0:3], v[192:207]\n\t" \
    "v_mfma_f32_32x32x16_bf16 v[208:223], v[68:71], v[0:3], v[208:223]\n\t" \
    "v_mfma_f32_32x32x16_bf16 v[192:207], v[72:75], v[4:7], v[192:207]\n\t" \
    "v_mfma_f32_32x32x16_bf16 v[208:223], v[76:79], v[4:7], v[208:223]\n\t" \
    "s_waitcnt vmcnt(12)\n\t" \
    "v_mfma_f32_32x32x16_bf16 v[192:207], v[80:83], v[8:11], v[192:207]\n\t" \
    "v_mfma_f32_32x32x16_bf16 v[208:223], v[84:87], v[8:11], v[208:223]\n\t" \
    "v_mfma_f32_32x32x16_bf16 v[192:207], v[88:91], v[12:15], v[192:207]\n\t" \
    "v_mfma_f32_32x32x16_bf16 v[208:223], v[92:95], v[12:15], v[208:223]\n\t" \
    "s_waitcnt vmcnt(10)\n\t" \
    "v_mfma_f32_32x32x16_bf16 v[192:207], v[96:99], v[16:19], v[192:207]\n\t" \
    "v_mfma_f32_32x32x16_bf16 v[208:223], v[100:103], v[16:19], v[208:223]\n\t" \
    "v_mfma_f32_32x32x16_bf16 v[192:207], v[104:107], v[20:23], v[192:207]\n\t" \
    "v_mfma_f32_32x32x16_bf16 v[208:223], v[108:111], v[20:23], v[208:223]\n\t" \
    "s_waitcnt vmcnt(8)\n\t" \
    "v_mfma_f32_32x32x16_bf16 v[192:207], v[112:115], v[24:27], v[192:207]\n\t" \
    "v_mfma_f32_32x32x16_bf16 v[208:223], v[116:119], v[24:27], v[208:223]\n\t" \
    "v_mfma_f32_32x32x16_bf16 v[192:207], v[120:123], v[28:31], v[192:207]\n\t" \
    "v_mfma_f32_32x32x16_bf16 v[208:223], v[124:127], v[28:31], v[208:223]\n\t" \
    "s_waitcnt vmcnt(6)\n\t" \
    "v_mfma_f32_32x32x16_bf16 v[192:207], v[128:131], v[32:35], v[192:207]\n\t" \
    "v_mfma_f32_32x32x16_bf16 v[208:223], v[132:135], v[32:35], v[208:223]\n\t" \
    "v_mfma_f32_32x32x16_bf16 v[192:207], v[136:139], v[36:39], v[192:207]\n\t" \
    "v_mfma_f32_32x32x16_bf16 v[208:223], v[140:143], v[36:39], v[208:223]\n\t" \
    "s_waitcnt vmcnt(4)\n\t" \
    "v_mfma_f32_32x32x16_bf16 v[192:207], v[144:147], v[40:43], v[192:207]\n\t" \
    "v_mfma_f32_32x32x16_bf16 v[208:223], v[148:151], v[40:43], v[208:223]\n\t" \
    "v_mfma_f32_32x32x16_bf16 v[192:207], v[152:155], v[44:47], v[192:207]\n\t" \
    "v_mfma_f32_32x32x16_bf16 v[208:223], v[156:159], v[44:47], v[208:223]\n\t" \
    "s_waitcnt vmcnt(2)\n\t" \
    "v_mfma_f32_32x32x16_bf16 v[192:207], v[160:163], v[48:51], v[192:207]\n\t" \
    "v_mfma_f32_32x32x16_bf16 v[208:223], v[164:167], v[48:51], v[208:223]\n\t" \
    "v_mfma_f32_32x32x16_bf16 v[192:207], v[168:171], v[52:55], v[192:207]\n\t" \
    "v_mfma_f32_32x32x16_bf16 v[208:223], v[172:175], v[52:55], v[208:223]\n\t" \
    "s_waitcnt vmcnt(0)\n\t" \
    "v_mfma_f32_32x32x16_bf16 v[192:207], v[176:179], v[56:59], v[192:207]\n\t" \
    "v_mfma_f32_32x32x16_bf16 v[208:223], v[180:183], v[56:59], v[208:223]\n\t" \
    "v_mfma_f32_32x32x16_bf16 v[192:207], v[184:187], v[60:63], v[192:207]\n\t" \
    "v_mfma_f32_32x32x16_bf16 v[208:223], v[188:191], v[60:63], v[208:223]\n\t" \
    "s_branch 3f\n\t" \
    "60:\n\t" \
    "s_mov_b32 %[t0], %[pend]\n\t" \
    "s_mov_b32 %[pend], 0\n\t" \
    "s_mov_b32 %[cap], 0x10000\n\t" \
    "s_branch 2f\n\t" \
    "1:\n\t" \
    "s_bcnt1_i32_b32 %[t0], %[pend]\n\t" \
    "s_cmp_eq_u32 %[t0], 8\n\t" \
    "s_cbranch_scc0 48f\n\t" \
    "s_waitcnt vmcnt(16)\n\t" \
    "s_branch 40f\n\t" \
    "48:\n\t" \
    "s_cmp_eq_u32 %[t0], 7\n\t" \
    "s_cbranch_scc0 47f\n\t" \
    "s_waitcnt vmcnt(14)\n\t" \
    "s_branch 40f\n\t" \
    "47:\n\t" \
    "s_cmp_eq_u32 %[t0], 6\n\t" \
    "s_cbranch_scc0 46f\n\t" \
    "s_waitcnt vmcnt(12)\n\t" \
    "s_branch 40f\n\t" \
    "46:\n\t" \
    "s_cmp_eq_u32 %[t0], 5\n\t" \
    "s_cbranch_scc0 45f\n\t" \
    "s_waitcnt vmcnt(10)\n\t" \
    "s_branch 40f\n\t" \
    "45:\n\t" \
    "s_cmp_eq_u32 %[t0], 4\n\t" \
    "s_cbranch_scc0 44f\n\t" \
    "s_waitcnt vmcnt(8)\n\t" \
    "s_branch 40f\n\t" \
    "44:\n\t" \
    "s_cmp_eq_u32 %[t0], 3\n\t" \
    "s_cbranch_scc0 43f\n\t" \
    "s_waitcnt vmcnt(6)\n\t" \
    "s_branch 40f\n\t" \
    "43:\n\t" \
    "s_cmp_eq_u32 %[t0], 2\n\t" \
    "s_cbranch_scc0 42f\n\t" \
    "s_waitcnt vmcnt(4)\n\t" \
    "s_branch 40f\n\t" \
    "42:\n\t" \
    "s_cmp_eq_u32 %[t0], 1\n\t" \
    "s_cbranch_scc0 41f\n\t" \
    "s_waitcnt vmcnt(2)\n\t" \
    "s_branch 40f\n\t" \
    "41:\n\t" \
    "s_waitcnt vmcnt(0)\n\t" \
    "40:\n\t" \
    "s_andn2_b32 %[lnd], %[iss], %[pend]\n\t" \
    "v_cmp_eq_u64 vcc, %[tag], v[224:225]\n\t" \
    "s_sub_u32 %[cap], %[cap], 1\n\t" \
    "s_cbranch_scc1 3f\n\t" \
    "s_nop 1\n\t" \
    "s_and_b32 %[t0], vcc_lo, 0xff\n\t" \
    "2:\n\t" \
    "s_andn2_b32 %[pend], %[t0], %[iss]\n\t" \
    "s_or_b32 %[iss], %[iss], %[pend]\n\t" \
    "s_cmp_eq_u32 %[iss], 0xff\n\t" \
    "s_cbranch_scc1 4f\n\t" \
    "global_load_dwordx2 v[224:225], %[fl], off sc1\n\t" \
    "4:\n\t" \
    "s_bitcmp1_b32 %[pend], 0\n\t" \
    "s_cbranch_scc0 20f\n\t" \
    "s_add_u32 %[t0], %[so], 0\n\t" \
    "buffer_load_dwordx4 v[0:3], %[vl], %[srd], %[t0] offen sc1\n\t" \
    "buffer_load_dwordx4 v[4:7], %[vl], %[srd], %[t0] offen offset:1024 sc1\n\t" \
    "20:\n\t" \
    "s_bitcmp1_b32 %[pend], 1\n\t" \
    "s_cbranch_scc0 21f\n\t" \
    "s_add_u32 %[t0], %[so], 2048\n\t" \
    "buffer_load_dwordx4 v[8:11], %[vl], %[srd], %[t0] offen sc1\n\t" \
    "buffer_load_dwordx4 v[12:15], %[vl], %[srd], %[t0] offen offset:1024 sc1\n\t" \
    "21:\n\t" \
    "s_bitcmp1_b32 %[pend], 2\n\t" \
    "s_cbranch_scc0 22f\n\t" \
    "s_add_u32 %[t0], %[so], 4096\n\t" \
    "buffer_load_dwordx4 v[16:19], %[vl], %[srd], %[t0] offen sc1\n\t" \
    "buffer_load_dwordx4 v[20:23], %[vl], %[srd], %[t0] offen offset:1024 sc1\n\t" \
    "22:\n\t" \
    "s_bitcmp1_b32 %[pend], 3\n\t" \
    "s_cbranch_scc0 23f\n\t" \
    "s_add_u32 %[t0], %[so], 6144\n\t" \
    "buffer_load_dwordx4 v[24:27], %[vl], %[srd], %[t0] offen sc1\n\t" \
    "buffer_load_dwordx4 v[28:31], %[vl], %[srd], %[t0] offen offset:1024 sc1\n\t" \
    "23:\n\t" \
    "s_bitcmp1_b32 %[pend], 4\n\t" \
    "s_cbranch_scc0 24f\n\t" \
    "s_add_u32 %[t0], %[so], 8192\n\t" \
    "buffer_load_dwordx4 v[32:35], %[vl], %[srd], %[t0] offen sc1\n\t" \
    "buffer_load_dwordx4 v[36:39], %[vl], %[srd], %[t0] offen offset:1024 sc1\n\t" \
    "24:\n\t" \
    "s_bitcmp1_b32 %[pend], 5\n\t" \
    "s_cbranch_scc0 25f\n\t" \
    "s_add_u32 %[t0], %[so], 10240\n\t" \
    "buffer_load_dwordx4 v[40:43], %[vl], %[srd], %[t0] offen sc1\n\t" \
    "buffer_load_dwordx4 v[44:47], %[vl], %[srd], %[t0] offen offset:1024 sc1\n\t" \
    "25:\n\t" \
    "s_bitcmp1_b32 %[pend], 6\n\t" \
    "s_cbranch_scc0 26f\n\t" \
    "s_add_u32 %[t0], %[so], 12288\n\t" \
    "buffer_load_dwordx4 v[48:51], %[vl], %[srd], %[t0] offen sc1\n\t" \
    "buffer_load_dwordx4 v[52:55], %[vl], %[srd], %[t0] offen offset:1024 sc1\n\t" \
    "26:\n\t" \
    "s_bitcmp1_b32 %[pend], 7\n\t" \
    "s_cbranch_scc0 27f\n\t" \
    "s_add_u32 %[t0], %[so], 14336\n\t" \
    "buffer_load_dwordx4 v[56:59], %[vl], %[srd], %[t0] offen sc1\n\t" \
    "buffer_load_dwordx4 v[60:63], %[vl], %[srd], %[t0] offen offset:1024 sc1\n\t" \
    "27:\n\t" \
    "s_cmp_lg_u32 %[nxt], 0\n\t" \
    "s_cbranch_scc1 10f\n\t" \
    "s_bitcmp1_b32 %[lnd], 0\n\t" \
    "s_cbranch_scc0 10f\n\t" \
    "v_mfma_f32_32x32x16_bf16 v[192:207], v[64:67], v[0:3], v[192:207]\n\t" \
    "v_mfma_f32_32x32x16_bf16 v[208:223], v[68:71], v[0:3], v[208:223]\n\t" \
    "v_mfma_f32_32x32x16_bf16 v[192:207], v[72:75], v[4:7], v[192:207]\n\t" \
    "v_mfma_f32_32x32x16_bf16 v[208:223], v[76:79], v[4:7], v[208:223]\n\t" \
    "s_mov_b32 %[nxt], 1\n\t" \
    "10:\n\t" \
    "s_cmp_lg_u32 %[nxt], 1\n\t" \
    "s_cbranch_scc1 11f\n\t" \
    "s_bitcmp1_b32 %[lnd], 1\n\t" \
    "s_cbranch_scc0 11f\n\t" \
    "v_mfma_f32_32x32x16_bf16 v[192:207], v[80:83], v[8:11], v[192:207]\n\t" \
    "v_mfma_f32_32x32x16_bf16 v[208:223], v[84:87], v[8:11], v[208:223]\n\t" \
    "v_mfma_f32_32x32x16_bf16 v[192:207], v[88:91], v[12:15], v[192:207]\n\t" \
    "v_mfma_f32_32x32x16_bf16 v[208:223], v[92:95], v[12:15], v[208:223]\n\t" \
    "s_mov_b32 %[nxt], 2\n\t" \
    "11:\n\t" \
    "s_cmp_lg_u32 %[nxt], 2\n\t" \
    "s_cbranch_scc1 12f\n\t" \
    "s_bitcmp1_b32 %[lnd], 2\n\t" \
    "s_cbranch_scc0 12f\n\t" \
    "v_mfma_f32_32x32x16_bf16 v[192:207], v[96:99], v[16:19], v[192:207]\n\t" \
    "v_mfma_f32_32x32x16_bf16 v[208:223], v[100:103], v[16:19], v[208:223]\n\t" \
    "v_mfma_f32_32x32x16_bf16 v[192:207], v[104:107], v[20:23], v[192:207]\n\t" \
    "v_mfma_f32_32x32x16_bf16 v[208:223], v[108:111], v[20:23], v[208:223]\n\t" \
    "s_mov_b32 %[nxt], 3\n\t" \
    "12:\n\t" \
    "s_cmp_lg_u32 %[nxt], 3\n\t" \
    "s_cbranch_scc1 13f\n\t" \
    "s_bitcmp1_b32 %[lnd], 3\n\t" \
    "s_cbranch_scc0 13f\n\t" \
    "v_mfma_f32_32x32x16_bf16 v[192:207], v[112:115], v[24:27], v[192:207]\n\t" \
    "v_mfma_f32_32x32x16_bf16 v[208:223], v[116:119], v[24:27], v[208:223]\n\t" \
    "v_mfma_f32_32x32x16_bf16 v[192:207], v[120:123], v[28:31], v[192:207]\n\t" \
    "v_mfma_f32_32x32x16_bf16 v[208:223], v[124:127], v[28:31], v[208:223]\n\t" \
    "s_mov_b32 %[nxt], 4\n\t" \
    "13:\n\t" \
    "s_cmp_lg_u32 %[nxt], 4\n\t" \
    "s_cbranch_scc1 14f\n\t" \
    "s_bitcmp1_b32 %[lnd], 4\n\t" \
    "s_cbranch_scc0 14f\n\t" \
    "v_mfma_f32_32x32x16_bf16 v[192:207], v[128:131], v[32:35], v[192:207]\n\t" \
    "v_mfma_f32_32x32x16_bf16 v[208:223], v[132:135], v[32:35], v[208:223]\n\t" \
    "v_mfma_f32_32x32x16_bf16 v[192:207], v[136:139], v[36:39], v[192:207]\n\t" \
    "v_mfma_f32_32x32x16_bf16 v[208:223], v[140:143], v[36:39], v[208:223]\n\t" \
    "s_mov_b32 %[nxt], 5\n\t" \
    "14:\n\t" \
    "s_cmp_lg_u32 %[nxt], 5\n\t" \
    "s_cbranch_scc1 15f\n\t" \
    "s_bitcmp1_b32 %[lnd], 5\n\t" \
    "s_cbranch_scc0 15f\n\t" \
    "v_mfma_f32_32x32x16_bf16 v[192:207], v[144:147], v[40:43], v[192:207]\n\t" \
    "v_mfma_f32_32x32x16_bf16 v[208:223], v[148:151], v[40:43], v[208:223]\n\t" \
    "v_mfma_f32_32x32x16_bf16 v[192:207], v[152:155], v[44:47], v[192:207]\n\t" \
    "v_mfma_f32_32x32x16_bf16 v[208:223], v[156:159], v[44:47], v[208:223]\n\t" \
    "s_mov_b32 %[nxt], 6\n\t" \
    "15:\n\t" \
    "s_cmp_lg_u32 %[nxt], 6\n\t" \
    "s_cbranch_scc1 16f\n\t" \
    "s_bitcmp1_b32 %[lnd], 6\n\t" \
    "s_cbranch_scc0 16f\n\t" \
    "v_mfma_f32_32x32x16_bf16 v[192:207], v[160:163], v[48:51], v[192:207]\n\t" \
    "v_mfma_f32_32x32x16_bf16 v[208:223], v[164:167], v[48:51], v[208:223]\n\t" \
    "v_mfma_f32_32x32x16_bf16 v[192:207], v[168:171], v[52:55], v[192:207]\n\t" \
    "v_mfma_f32_32x32x16_bf16 v[208:223], v[172:175], v[52:55], v[208:223]\n\t" \
    "s_mov_b32 %[nxt], 7\n\t" \
    "16:\n\t" \
    "s_cmp_lg_u32 %[nxt], 7\n\t" \
    "s_cbranch_scc1 17f\n\t" \
    "s_bitcmp1_b32 %[lnd], 7\n\t" \
    "s_cbranch_scc0 17f\n\t" \
    "v_mfma_f32_32x32x16_bf16 v[192:207], v[176:179], v[56:59], v[192:207]\n\t" \
    "v_mfma_f32_32x32x16_bf16 v[208:223], v[180:183], v[56:59], v[208:223]\n\t" \
    "v_mfma_f32_32x32x16_bf16 v[192:207], v[184:187], v[60:63], v[192:207]\n\t" \
    "v_mfma_f32_32x32x16_bf16 v[208:223], v[188:191], v[60:63], v[208:223]\n\t" \
    "s_mov_b32 %[nxt], 8\n\t" \
    "17:\n\t" \
    "s_cmp_eq_u32 %[iss], 0xff\n\t" \
    "s_cbranch_scc0 1b\n\t" \
    "s_waitcnt vmcnt(0)\n\t" \
    "s_mov_b32 %[lnd], 0xff\n\t" \
    "s_cmp_lg_u32 %[nxt], 0\n\t" \
    "s_cbranch_scc1 50f\n\t" \
    "s_bitcmp1_b32 %[lnd], 0\n\t" \
    "s_cbranch_scc0 50f\n\t" \
    "v_mfma_f32_32x32x16_bf16 v[192:207], v[64:67], v[0:3], v[192:207]\n\t" \
    "v_mfma_f32_32x32x16_bf16 v[208:223], v[68:71], v[0:3], v[208:223]\n\t" \
    "v_mfma_f32_32x32x16_bf16 v[192:207], v[72:75], v[4:7], v[192:207]\n\t" \
    "v_mfma_f32_32x32x16_bf16 v[208:223], v[76:79], v[4:7], v[208:223]\n\t" \
    "s_mov_b32 %[nxt], 1\n\t" \
    "50:\n\t" \
    "s_cmp_lg_u32 %[nxt], 1\n\t" \
    "s_cbranch_scc1 51f\n\t" \
    "s_bitcmp1_b32 %[lnd], 1\n\t" \
    "s_cbranch_scc0 51f\n\t" \
    "v_mfma_f32_32x32x16_bf16 v[192:207], v[80:83], v[8:11], v[192:207]\n\t" \
    "v_mfma_f32_32x32x16_bf16 v[208:223], v[84:87], v[8:11], v[208:223]\n\t" \
    "v_mfma_f32_32x32x16_bf16 v[192:207], v[88:91], v[12:15], v[192:207]\n\t" \
    "v_mfma_f32_32x32x16_bf16 v[208:223], v[92:95], v[12:15], v[208:223]\n\t" \
    "s_mov_b32 %[nxt], 2\n\t" \
    "51:\n\t" \
    "s_cmp_lg_u32 %[nxt], 2\n\t" \
    "s_cbranch_scc1 52f\n\t" \
    "s_bitcmp1_b32 %[lnd], 2\n\t" \
    "s_cbranch_scc0 52f\n\t" \
    "v_mfma_f32_32x32x16_bf16 v[192:207], v[96:99], v[16:19], v[192:207]\n\t" \
    "v_mfma_f32_32x32x16_bf16 v[208:223], v[100:103], v[16:19], v[208:223]\n\t" \
    "v_mfma_f32_32x32x16_bf16 v[192:207], v[104:107], v[20:23], v[192:207]\n\t" \
    "v_mfma_f32_32x32x16_bf16 v[208:223], v[108:111], v[20:23], v[208:223]\n\t" \
    "s_mov_b32 %[nxt], 3\n\t" \
    "52:\n\t" \
    "s_cmp_lg_u32 %[nxt], 3\n\t" \
    "s_cbranch_scc1 53f\n\t" \
    "s_bitcmp1_b32 %[lnd], 3\n\t" \
    "s_cbranch_scc0 53f\n\t" \
    "v_mfma_f32_32x32x16_bf16 v[192:207], v[112:115], v[24:27], v[192:207]\n\t" \
    "v_mfma_f32_32x32x16_bf16 v[208:223], v[116:119], v[24:27], v[208:223]\n\t" \
    "v_mfma_f32_32x32x16_bf16 v[192:207], v[120:123], v[28:31], v[192:207]\n\t" \
    "v_mfma_f32_32x32x16_bf16 v[208:223], v[124:127], v[28:31], v[208:223]\n\t" \
    "s_mov_b32 %[nxt], 4\n\t" \
    "53:\n\t" \
    "s_cmp_lg_u32 %[nxt], 4\n\t" \
    "s_cbranch_scc1 54f\n\t" \
    "s_bitcmp1_b32 %[lnd], 4\n\t" \
    "s_cbranch_scc0 54f\n\t" \
    "v_mfma_f32_32x32x16_bf16 v[192:207], v[128:131], v[32:35], v[192:207]\n\t" \
    "v_mfma_f32_32x32x16_bf16 v[208:223], v[132:135], v[32:35], v[208:223]\n\t" \
    "v_mfma_f32_32x32x16_bf16 v[192:207], v[136:139], v[36:39], v[192:207]\n\t" \
    "v_mfma_f32_32x32x16_bf16 v[208:223], v[140:143], v[36:39], v[208:223]\n\t" \
    "s_mov_b32 %[nxt], 5\n\t" \
    "54:\n\t" \
    "s_cmp_lg_u32 %[nxt], 5\n\t" \
    "s_cbranch_scc1 55f\n\t" \
    "s_bitcmp1_b32 %[lnd], 5\n\t" \
    "s_cbranch_scc0 55f\n\t" \
    "v_mfma_f32_32x32x16_bf16 v[192:207], v[144:147], v[40:43], v[192:207]\n\t" \
    "v_mfma_f32_32x32x16_bf16 v[208:223], v[148:151], v[40:43], v[208:223]\n\t" \
    "v_mfma_f32_32x32x16_bf16 v[192:207], v[152:155], v[44:47], v[192:207]\n\t" \
    "v_mfma_f32_32x32x16_bf16 v[208:223], v[156:159], v[44:47], v[208:223]\n\t" \
    "s_mov_b32 %[nxt], 6\n\t" \
    "55:\n\t" \
    "s_cmp_lg_u32 %[nxt], 6\n\t" \
    "s_cbranch_scc1 56f\n\t" \
    "s_bitcmp1_b32 %[lnd], 6\n\t" \
    "s_cbranch_scc0 56f\n\t" \
    "v_mfma_f32_32x32x16_bf16 v[192:207], v[160:163], v[48:51], v[192:207]\n\t" \
    "v_mfma_f32_32x32x16_bf16 v[208:223], v[164:167], v[48:51], v[208:223]\n\t" \
    "v_mfma_f32_32x32x16_bf16 v[192:207], v[168:171], v[52:55], v[192:207]\n\t" \
    "v_mfma_f32_32x32x16_bf16 v[208:223], v[172:175], v[52:55], v[208:223]\n\t" \
    "s_mov_b32 %[nxt], 7\n\t" \
    "56:\n\t" \
    "s_cmp_lg_u32 %[nxt], 7\n\t" \
    "s_cbranch_scc1 57f\n\t" \
    "s_bitcmp1_b32 %[lnd], 7\n\t" \
    "s_cbranch_scc0 57f\n\t" \
    "v_mfma_f32_32x32x16_bf16 v[192:207], v[176:179], v[56:59], v[192:207]\n\t" \
    "v_mfma_f32_32x32x16_bf16 v[208:223], v[180:183], v[56:59], v[208:223]\n\t" \
    "v_mfma_f32_32x32x16_bf16 v[192:207], v[184:187], v[60:63], v[192:207]\n\t" \
    "v_mfma_f32_32x32x16_bf16 v[208:223], v[188:191], v[60:63], v[208:223]\n\t" \
    "s_mov_b32 %[nxt], 8\n\t" \
    "57:\n\t" \
    "3:\n\t" \
    "s_nop 7\n\t" \
    "s_nop 7\n\t" \
    "s_cmp_eq_u32 %[kq], 0\n\t" \
    "s_cbranch_scc1 64f\n\t" \
    "ds_write_b128 %[sm], v[192:195] offset:0\n\t" \
    "ds_write_b128 %[sm], v[196:199] offset:1024\n\t" \
    "ds_write_b128 %[sm], v[200:203] offset:2048\n\t" \
    "ds_write_b128 %[sm], v[204:207] offset:3072\n\t" \
    "v_mov_b64 v[192:193], v[208:209]\n\t" \
    "v_mov_b64 v[194:195], v[210:211]\n\t" \
    "v_mov_b64 v[196:197], v[212:213]\n\t" \
    "v_mov_b64 v[198:199], v[214:215]\n\t" \
    "v_mov_b64 v[200:201], v[216:217]\n\t" \
    "v_mov_b64 v[202:203], v[218:219]\n\t" \
    "v_mov_b64 v[204:205], v[220:221]\n\t" \
    "v_mov_b64 v[206:207], v[222:223]\n\t" \
    "s_branch 65f\n\t" \
    "64:\n\t" \
    "ds_write_b128 %[sm], v[208:211] offset:0\n\t" \
    "ds_write_b128 %[sm], v[212:215] offset:1024\n\t" \
    "ds_write_b128 %[sm], v[216:219] offset:2048\n\t" \
    "ds_write_b128 %[sm], v[220:223] offset:3072\n\t" \
    "65:\n\t" \
    "v_mov_b32 v224, 1\n\t" \
    "s_waitcnt lgkmcnt(0)\n\t" \
    "s_mov_b64 exec, 1\n\t" \
    "ds_write_b32 %[fm], v224\n\t" \
    "s_mov_b64 exec, -1\n\t" \
    "s_mov_b32 %[cap], 0x10000\n\t" \
    "66:\n\t" \
    "ds_read_b32 v225, %[fp]\n\t" \
    "s_waitcnt lgkmcnt(0)\n\t" \
    "v_readfirstlane_b32 %[t0], v225\n\t" \
    "s_sub_u32 %[cap], %[cap], 1\n\t" \
    "s_cbranch_scc1 67f\n\t" \
    "s_cmp_lt_u32 %[t0], 1\n\t" \
    "s_cbranch_scc1 66b\n\t" \
    "67:\n\t" \
    "ds_read_b128 v[0:3], %[sp] offset:0\n\t" \
    "ds_read_b128 v[4:7], %[sp] offset:1024\n\t" \
    "ds_read_b128 v[8:11], %[sp] offset:2048\n\t" \
    "ds_read_b128 v[12:15], %[sp] offset:3072\n\t" \
    "s_waitcnt lgkmcnt(0)\n\t" \
    "v_pk_add_f32 v[192:193], v[192:193], v[0:1]\n\t" \
    "v_pk_add_f32 v[194:195], v[194:195], v[2:3]\n\t" \
    "v_pk_add_f32 v[196:197], v[196:197], v[4:5]\n\t" \
    "v_pk_add_f32 v[198:199], v[198:199], v[6:7]\n\t" \
    "v_pk_add_f32 v[200:201], v[200:201], v[8:9]\n\t" \
    "v_pk_add_f32 v[202:203], v[202:203], v[10:11]\n\t" \
    "v_pk_add_f32 v[204:205], v[204:205], v[12:13]\n\t" \
    "v_pk_add_f32 v[206:207], v[206:207], v[14:15]\n\t" \
    "v_cvt_pk_bf16_f32 v0, v192, v193\n\t" \
    "v_cvt_pk_bf16_f32 v1, v194, v195\n\t" \
    "v_cvt_pk_bf16_f32 v2, v196, v197\n\t" \
    "v_cvt_pk_bf16_f32 v3, v198, v199\n\t" \
    "v_cvt_pk_bf16_f32 v4, v200, v201\n\t" \
    "v_cvt_pk_bf16_f32 v5, v202, v203\n\t" \
    "v_cvt_pk_bf16_f32 v6, v204, v205\n\t" \
    "v_cvt_pk_bf16_f32 v7, v206, v207\n\t" \
    "s_nop 1\n\t" \
    "v_permlane32_swap_b32 v0, v2\n\t" \
    "v_permlane32_swap_b32 v1, v3\n\t" \
    "v_permlane32_swap_b32 v4, v6\n\t" \
    "v_permlane32_swap_b32 v5, v7\n\t" \
    "global_store_dwordx4 %[out], v[0:3], off\n\t" \
    "global_store_dwordx4 %[out], v[4:7], off offset:32\n\t" \
    "s_nop 1\n\t" \
    ""
#define OUTPROJ_OWN \
    "global_load_dwordx2 v[224:225], %[fl], off sc1\n\t" \
    "v_mov_b64 v[192:193], 0\n\t" \
    "v_mov_b64 v[194:195], 0\n\t" \
    "v_mov_b64 v[196:197], 0\n\t" \
    "v_mov_b64 v[198:199], 0\n\t" \
    "v_mov_b64 v[200:201], 0\n\t" \
    "v_mov_b64 v[202:203], 0\n\t" \
    "v_mov_b64 v[204:205], 0\n\t" \
    "v_mov_b64 v[206:207], 0\n\t" \
    "v_mov_b64 v[208:209], 0\n\t" \
    "v_mov_b64 v[210:211], 0\n\t" \
    "v_mov_b64 v[212:213], 0\n\t" \
    "v_mov_b64 v[214:215], 0\n\t" \
    "v_mov_b64 v[216:217], 0\n\t" \
    "v_mov_b64 v[218:219], 0\n\t" \
    "v_mov_b64 v[220:221], 0\n\t" \
    "v_mov_b64 v[222:223], 0\n\t" \
    "s_mov_b32 %[iss], 0\n\t" \
    "s_mov_b32 %[nxt], 0\n\t" \
    "s_mov_b32 %[lnd], 0\n\t" \
    "s_mov_b32 %[pend], 0\n\t" \
    "s_waitcnt vmcnt(0)\n\t" \
    "v_mov_b32 v226, %[taglo]\n\t" \
    "v_mov_b32 v227, %[taghi]\n\t" \
    "s_mov_b64 exec, 1\n\t" \
    "global_store_dwordx2 %[pub], v[226:227], off sc1\n\t" \
    "s_mov_b64 exec, -1\n\t" \
    "v_cmp_eq_u64 vcc, %[tag], v[224:225]\n\t" \
    "s_nop 1\n\t" \
    "s_and_b32 %[t0], vcc_lo, 0xff\n\t" \
    "s_mov_b32 %[pend], %[t0]\n\t" \
    "s_cmp_eq_u32 %[t0], 0xff\n\t" \
    "s_cbranch_scc0 60f\n\t" \
    "s_add_u32 %[t0], %[so], 0\n\t" \
    "buffer_load_dwordx4 v[0:3], %[vl], %[srd], %[t0] offen sc1\n\t" \
    "buffer_load_dwordx4 v[4:7], %[vl], %[srd], %[t0] offen offset:1024 sc1\n\t" \
    "s_add_u32 %[t0], %[so], 2048\n\t" \
    "buffer_load_dwordx4 v[8:11], %[vl], %[srd], %[t0] offen sc1\n\t" \
    "buffer_load_dwordx4 v[12:15], %[vl], %[srd], %[t0] offen offset:1024 sc1\n\t" \
    "s_add_u32 %[t0], %[so], 4096\n\t" \
    "buffer_load_dwordx4 v[16:19], %[vl], %[srd], %[t0] offen sc1\n\t" \
    "buffer_load_dwordx4 v[20:23], %[vl], %[srd], %[t0] offen offset:1024 sc1\n\t" \
    "s_add_u32 %[t0], %[so], 6144\n\t" \
    "buffer_load_dwordx4 v[24:27], %[vl], %[srd], %[t0] offen sc1\n\t" \
    "buffer_load_dwordx4 v[28:31], %[vl], %[srd], %[t0] offen offset:1024 sc1\n\t" \
    "s_add_u32 %[t0], %[so], 8192\n\t" \
    "buffer_load_dwordx4 v[32:35], %[vl], %[srd], %[t0] offen sc1\n\t" \
    "buffer_load_dwordx4 v[36:39], %[vl], %[srd], %[t0] offen offset:1024 sc1\n\t" \
    "s_add_u32 %[t0], %[so], 10240\n\t" \
    "buffer_load_dwordx4 v[40:43], %[vl], %[srd], %[t0] offen sc1\n\t" \
    "buffer_load_dwordx4 v[44:47], %[vl], %[srd], %[t0] offen offset:1024 sc1\n\t" \
    "s_add_u32 %[t0], %[so], 12288\n\t" \
    "buffer_load_dwordx4 v[48:51], %[vl], %[srd], %[t0] offen sc1\n\t" \
    "buffer_load_dwordx4 v[52:55], %[vl], %[srd], %[t0] offen offset:1024 sc1\n\t" \
    "s_add_u32 %[t0], %[so], 14336\n\t" \
    "buffer_load_dwordx4 v[56:59], %[vl], %[srd], %[t0] offen sc1\n\t" \
    "buffer_load_dwordx4 v[60:63], %[vl], %[srd], %[t0] offen offset:1024 sc1\n\t" \
    "s_mov_b32 %[cap], 0x10000\n\t" \
    "70:\n\t" \
    "ds_read_b32 v225, %[w2]\n\t" \
    "s_waitcnt lgkmcnt(0)\n\t" \
    "v_readfirstlane_b32 %[t0], v225\n\t" \
    "s_sub_u32 %[cap], %[cap], 1\n\t" \
    "s_cbranch_scc1 71f\n\t" \
    "s_cmp_lt_u32 %[t0], 4\n\t" \
    "s_cbranch_scc1 70b\n\t" \
    "71:\n\t" \
    "ds_read_b64_tr_b16 v[64:65], %[wa0] offset:0\n\t" \
    "ds_read_b64_tr_b16 v[66:67], %[wa0] offset:1024\n\t" \
    "ds_read_b64_tr_b16 v[72:73], %[wa0] offset:2048\n\t" \
    "ds_read_b64_tr_b16 v[74:75], %[wa0] offset:3072\n\t" \
    "ds_read_b64_tr_b16 v[80:81], %[wa0] offset:3584\n\t" \
    "ds_read_b64_tr_b16 v[82:83], %[wa0] offset:4608\n\t" \
    "ds_read_b64_tr_b16 v[88:89], %[wa0] offset:5632\n\t" \
    "ds_read_b64_tr_b16 v[90:91], %[wa0] offset:6656\n\t" \
    "ds_read_b64_tr_b16 v[96:97], %[wa0] offset:7168\n\t" \
    "ds_read_b64_tr_b16 v[98:99], %[wa0] offset:8192\n\t" \
    "ds_read_b64_tr_b16 v[104:105], %[wa0] offset:9216\n\t" \
    "ds_read_b64_tr_b16 v[106:107], %[wa0] offset:10240\n\t" \
    "ds_read_b64_tr_b16 v[112:113], %[wa0] offset:10752\n\t" \
    "ds_read_b64_tr_b16 v[114:115], %[wa0] offset:11776\n\t" \
    "ds_read_b64_tr_b16 v[120:121], %[wa0] offset:12800\n\t" \
    "ds_read_b64_tr_b16 v[122:123], %[wa0] offset:13824\n\t" \
    "ds_read_b64_tr_b16 v[128:129], %[wa0] offset:14336\n\t" \
    "ds_read_b64_tr_b16 v[130:131], %[wa0] offset:15360\n\t" \
    "ds_read_b64_tr_b16 v[136:137], %[wa0] offset:16384\n\t" \
    "ds_read_b64_tr_b16 v[138:139], %[wa0] offset:17408\n\t" \
    "ds_read_b64_tr_b16 v[144:145], %[wa0] offset:17920\n\t" \
    "ds_read_b64_tr_b16 v[146:147], %[wa0] offset:18944\n\t" \
    "ds_read_b64_tr_b16 v[152:153], %[wa0] offset:19968\n\t" \
    "ds_read_b64_tr_b16 v[154:155], %[wa0] offset:20992\n\t" \
    "ds_read_b64_tr_b16 v[160:161], %[wa0] offset:21504\n\t" \
    "ds_read_b64_tr_b16 v[162:163], %[wa0] offset:22528\n\t" \
    "ds_read_b64_tr_b16 v[168:169], %[wa0] offset:23552\n\t" \
    "ds_read_b64_tr_b16 v[170:171], %[wa0] offset:24576\n\t" \
    "ds_read_b64_tr_b16 v[176:177], %[wa0] offset:25088\n\t" \
    "ds_read_b64_tr_b16 v[178:179], %[wa0] offset:26112\n\t" \
    "ds_read_b64_tr_b16 v[184:185], %[wa0] offset:27136\n\t" \
    "ds_read_b64_tr_b16 v[186:187], %[wa0] offset:28160\n\t" \
    "ds_read_b64_tr_b16 v[68:69], %[wa1] offset:0\n\t" \
    "ds_read_b64_tr_b16 v[70:71], %[wa1] offset:1024\n\t" \
    "ds_read_b64_tr_b16 v[76:77], %[wa1] offset:2048\n\t" \
    "ds_read_b64_tr_b16 v[78:79], %[wa1] offset:3072\n\t" \
    "ds_read_b64_tr_b16 v[84:85], %[wa1] offset:3584\n\t" \
    "ds_read_b64_tr_b16 v[86:87], %[wa1] offset:4608\n\t" \
    "ds_read_b64_tr_b16 v[92:93], %[wa1] offset:5632\n\t" \
    "ds_read_b64_tr_b16 v[94:95], %[wa1] offset:6656\n\t" \
    "ds_read_b64_tr_b16 v[100:101], %[wa1] offset:7168\n\t" \
    "ds_read_b64_tr_b16 v[102:103], %[wa1] offset:8192\n\t" \
    "ds_read_b64_tr_b16 v[108:109], %[wa1] offset:9216\n\t" \
    "ds_read_b64_tr_b16 v[110:111], %[wa1] offset:10240\n\t" \
    "ds_read_b64_tr_b16 v[116:117], %[wa1] offset:10752\n\t" \
    "ds_read_b64_tr_b16 v[118:119], %[wa1] offset:11776\n\t" \
    "ds_read_b64_tr_b16 v[124:125], %[wa1] offset:12800\n\t" \
    "ds_read_b64_tr_b16 v[126:127], %[wa1] offset:13824\n\t" \
    "ds_read_b64_tr_b16 v[132:133], %[wa1] offset:14336\n\t" \
    "ds_read_b64_tr_b16 v[134:135], %[wa1] offset:15360\n\t" \
    "ds_read_b64_tr_b16 v[140:141], %[wa1] offset:16384\n\t" \
    "ds_read_b64_tr_b16 v[142:143], %[wa1] offset:17408\n\t" \
    "ds_read_b64_tr_b16 v[148:149], %[wa1] offset:17920\n\t" \
    "ds_read_b64_tr_b16 v[150:151], %[wa1] offset:18944\n\t" \
    "ds_read_b64_tr_b16 v[156:157], %[wa1] offset:19968\n\t" \
    "ds_read_b64_tr_b16 v[158:159], %[wa1] offset:20992\n\t" \
    "ds_read_b64_tr_b16 v[164:165], %[wa1] offset:21504\n\t" \
    "ds_read_b64_tr_b16 v[166:167], %[wa1] offset:22528\n\t" \
    "ds_read_b64_tr_b16 v[172:173], %[wa1] offset:23552\n\t" \
    "ds_read_b64_tr_b16 v[174:175], %[wa1] offset:24576\n\t" \
    "ds_read_b64_tr_b16 v[180:181], %[wa1] offset:25088\n\t" \
    "ds_read_b64_tr_b16 v[182:183], %[wa1] offset:26112\n\t" \
    "ds_read_b64_tr_b16 v[188:189], %[wa1] offset:27136\n\t" \
    "ds_read_b64_tr_b16 v[190:191], %[wa1] offset:28160\n\t" \
    "s_waitcnt lgkmcnt(0)\n\t" \
    "s_waitcnt vmcnt(14)\n\t" \
    "v_mfma_f32_32x32x16_bf16 v[192:207], v[64:67], v[0:3], v[192:207]\n\t" \
    "v_mfma_f32_32x32x16_bf16 v[208:223], v[68:71], v[0:3], v[208:223]\n\t" \
    "v_mfma_f32_32x32x16_bf16 v[192:207], v[72:75], v[4:7], v[192:207]\n\t" \
    "v_mfma_f32_32x32x16_bf16 v[208:223], v[76:79], v[4:7], v[208:223]\n\t" \
    "s_waitcnt vmcnt(12)\n\t" \
    "v_mfma_f32_32x32x16_bf16 v[192:207], v[80:83], v[8:11], v[192:207]\n\t" \
    "v_mfma_f32_32x32x16_bf16 v[208:223], v[84:87], v[8:11], v[208:223]\n\t" \
    "v_mfma_f32_32x32x16_bf16 v[192:207], v[88:91], v[12:15], v[192:207]\n\t" \
    "v_mfma_f32_32x32x16_bf16 v[208:223], v[92:95], v[12:15], v[208:223]\n\t" \
    "s_waitcnt vmcnt(10)\n\t" \
    "v_mfma_f32_32x32x16_bf16 v[192:207], v[96:99], v[16:19], v[192:207]\n\t" \
    "v_mfma_f32_32x32x16_bf16 v[208:223], v[100:103], v[16:19], v[208:223]\n\t" \
    "v_mfma_f32_32x32x16_bf16 v[192:207], v[104:107], v[20:23], v[192:207]\n\t" \
    "v_mfma_f32_32x32x16_bf16 v[208:223], v[108:111], v[20:23], v[208:223]\n\t" \
    "s_waitcnt vmcnt(8)\n\t" \
    "v_mfma_f32_32x32x16_bf16 v[192:207], v[112:115], v[24:27], v[192:207]\n\t" \
    "v_mfma_f32_32x32x16_bf16 v[208:223], v[116:119], v[24:27], v[208:223]\n\t" \
    "v_mfma_f32_32x32x16_bf16 v[192:207], v[120:123], v[28:31], v[192:207]\n\t" \
    "v_mfma_f32_32x32x16_bf16 v[208:223], v[124:127], v[28:31], v[208:223]\n\t" \
    "s_waitcnt vmcnt(6)\n\t" \
    "v_mfma_f32_32x32x16_bf16 v[192:207], v[128:131], v[32:35], v[192:207]\n\t" \
    "v_mfma_f32_32x32x16_bf16 v[208:223], v[132:135], v[32:35], v[208:223]\n\t" \
    "v_mfma_f32_32x32x16_bf16 v[192:207], v[136:139], v[36:39], v[192:207]\n\t" \
    "v_mfma_f32_32x32x16_bf16 v[208:223], v[140:143], v[36:39], v[208:223]\n\t" \
    "s_waitcnt vmcnt(4)\n\t" \
    "v_mfma_f32_32x32x16_bf16 v[192:207], v[144:147], v[40:43], v[192:207]\n\t" \
    "v_mfma_f32_32x32x16_bf16 v[208:223], v[148:151], v[40:43], v[208:223]\n\t" \
    "v_mfma_f32_32x32x16_bf16 v[192:207], v[152:155], v[44:47], v[192:207]\n\t" \
    "v_mfma_f32_32x32x16_bf16 v[208:223], v[156:159], v[44:47], v[208:223]\n\t" \
    "s_waitcnt vmcnt(2)\n\t" \
    "v_mfma_f32_32x32x16_bf16 v[192:207], v[160:163], v[48:51], v[192:207]\n\t" \
    "v_mfma_f32_32x32x16_bf16 v[208:223], v[164:167], v[48:51], v[208:223]\n\t" \
    "v_mfma_f32_32x32x16_bf16 v[192:207], v[168:171], v[52:55], v[192:207]\n\t" \
    "v_mfma_f32_32x32x16_bf16 v[208:223], v[172:175], v[52:55], v[208:223]\n\t" \
    "s_waitcnt vmcnt(0)\n\t" \
    "v_mfma_f32_32x32x16_bf16 v[192:207], v[176:179], v[56:59], v[192:207]\n\t" \
    "v_mfma_f32_32x32x16_bf16 v[208:223], v[180:183], v[56:59], v[208:223]\n\t" \
    "v_mfma_f32_32x32x16_bf16 v[192:207], v[184:187], v[60:63], v[192:207]\n\t" \
    "v_mfma_f32_32x32x16_bf16 v[208:223], v[188:191], v[60:63], v[208:223]\n\t" \
    "s_branch 3f\n\t" \
    "60:\n\t" \
    "s_mov_b32 %[cap], 0x10000\n\t" \
    "72:\n\t" \
    "ds_read_b32 v225, %[w2]\n\t" \
    "s_waitcnt lgkmcnt(0)\n\t" \
    "v_readfirstlane_b32 %[t0], v225\n\t" \
    "s_sub_u32 %[cap], %[cap], 1\n\t" \
    "s_cbranch_scc1 73f\n\t" \
    "s_cmp_lt_u32 %[t0], 4\n\t" \
    "s_cbranch_scc1 72b\n\t" \
    "73:\n\t" \
    "ds_read_b64_tr_b16 v[64:65], %[wa0] offset:0\n\t" \
    "ds_read_b64_tr_b16 v[66:67], %[wa0] offset:1024\n\t" \
    "ds_read_b64_tr_b16 v[72:73], %[wa0] offset:2048\n\t" \
    "ds_read_b64_tr_b16 v[74:75], %[wa0] offset:3072\n\t" \
    "ds_read_b64_tr_b16 v[80:81], %[wa0] offset:3584\n\t" \
    "ds_read_b64_tr_b16 v[82:83], %[wa0] offset:4608\n\t" \
    "ds_read_b64_tr_b16 v[88:89], %[wa0] offset:5632\n\t" \
    "ds_read_b64_tr_b16 v[90:91], %[wa0] offset:6656\n\t" \
    "ds_read_b64_tr_b16 v[96:97], %[wa0] offset:7168\n\t" \
    "ds_read_b64_tr_b16 v[98:99], %[wa0] offset:8192\n\t" \
    "ds_read_b64_tr_b16 v[104:105], %[wa0] offset:9216\n\t" \
    "ds_read_b64_tr_b16 v[106:107], %[wa0] offset:10240\n\t" \
    "ds_read_b64_tr_b16 v[112:113], %[wa0] offset:10752\n\t" \
    "ds_read_b64_tr_b16 v[114:115], %[wa0] offset:11776\n\t" \
    "ds_read_b64_tr_b16 v[120:121], %[wa0] offset:12800\n\t" \
    "ds_read_b64_tr_b16 v[122:123], %[wa0] offset:13824\n\t" \
    "ds_read_b64_tr_b16 v[128:129], %[wa0] offset:14336\n\t" \
    "ds_read_b64_tr_b16 v[130:131], %[wa0] offset:15360\n\t" \
    "ds_read_b64_tr_b16 v[136:137], %[wa0] offset:16384\n\t" \
    "ds_read_b64_tr_b16 v[138:139], %[wa0] offset:17408\n\t" \
    "ds_read_b64_tr_b16 v[144:145], %[wa0] offset:17920\n\t" \
    "ds_read_b64_tr_b16 v[146:147], %[wa0] offset:18944\n\t" \
    "ds_read_b64_tr_b16 v[152:153], %[wa0] offset:19968\n\t" \
    "ds_read_b64_tr_b16 v[154:155], %[wa0] offset:20992\n\t" \
    "ds_read_b64_tr_b16 v[160:161], %[wa0] offset:21504\n\t" \
    "ds_read_b64_tr_b16 v[162:163], %[wa0] offset:22528\n\t" \
    "ds_read_b64_tr_b16 v[168:169], %[wa0] offset:23552\n\t" \
    "ds_read_b64_tr_b16 v[170:171], %[wa0] offset:24576\n\t" \
    "ds_read_b64_tr_b16 v[176:177], %[wa0] offset:25088\n\t" \
    "ds_read_b64_tr_b16 v[178:179], %[wa0] offset:26112\n\t" \
    "ds_read_b64_tr_b16 v[184:185], %[wa0] offset:27136\n\t" \
    "ds_read_b64_tr_b16 v[186:187], %[wa0] offset:28160\n\t" \
    "ds_read_b64_tr_b16 v[68:69], %[wa1] offset:0\n\t" \
    "ds_read_b64_tr_b16 v[70:71], %[wa1] offset:1024\n\t" \
    "ds_read_b64_tr_b16 v[76:77], %[wa1] offset:2048\n\t" \
    "ds_read_b64_tr_b16 v[78:79], %[wa1] offset:3072\n\t" \
    "ds_read_b64_tr_b16 v[84:85], %[wa1] offset:3584\n\t" \
    "ds_read_b64_tr_b16 v[86:87], %[wa1] offset:4608\n\t" \
    "ds_read_b64_tr_b16 v[92:93], %[wa1] offset:5632\n\t" \
    "ds_read_b64_tr_b16 v[94:95], %[wa1] offset:6656\n\t" \
    "ds_read_b64_tr_b16 v[100:101], %[wa1] offset:7168\n\t" \
    "ds_read_b64_tr_b16 v[102:103], %[wa1] offset:8192\n\t" \
    "ds_read_b64_tr_b16 v[108:109], %[wa1] offset:9216\n\t" \
    "ds_read_b64_tr_b16 v[110:111], %[wa1] offset:10240\n\t" \
    "ds_read_b64_tr_b16 v[116:117], %[wa1] offset:10752\n\t" \
    "ds_read_b64_tr_b16 v[118:119], %[wa1] offset:11776\n\t" \
    "ds_read_b64_tr_b16 v[124:125], %[wa1] offset:12800\n\t" \
    "ds_read_b64_tr_b16 v[126:127], %[wa1] offset:13824\n\t" \
    "ds_read_b64_tr_b16 v[132:133], %[wa1] offset:14336\n\t" \
    "ds_read_b64_tr_b16 v[134:135], %[wa1] offset:15360\n\t" \
    "ds_read_b64_tr_b16 v[140:141], %[wa1] offset:16384\n\t" \
    "ds_read_b64_tr_b16 v[142:143], %[wa1] offset:17408\n\t" \
    "ds_read_b64_tr_b16 v[148:149], %[wa1] offset:17920\n\t" \
    "ds_read_b64_tr_b16 v[150:151], %[wa1] offset:18944\n\t" \
    "ds_read_b64_tr_b16 v[156:157], %[wa1] offset:19968\n\t" \
    "ds_read_b64_tr_b16 v[158:159], %[wa1] offset:20992\n\t" \
    "ds_read_b64_tr_b16 v[164:165], %[wa1] offset:21504\n\t" \
    "ds_read_b64_tr_b16 v[166:167], %[wa1] offset:22528\n\t" \
    "ds_read_b64_tr_b16 v[172:173], %[wa1] offset:23552\n\t" \
    "ds_read_b64_tr_b16 v[174:175], %[wa1] offset:24576\n\t" \
    "ds_read_b64_tr_b16 v[180:181], %[wa1] offset:25088\n\t" \
    "ds_read_b64_tr_b16 v[182:183], %[wa1] offset:26112\n\t" \
    "ds_read_b64_tr_b16 v[188:189], %[wa1] offset:27136\n\t" \
    "ds_read_b64_tr_b16 v[190:191], %[wa1] offset:28160\n\t" \
    "s_waitcnt lgkmcnt(0)\n\t" \
    "s_mov_b32 %[t0], %[pend]\n\t" \
    "s_mov_b32 %[pend], 0\n\t" \
    "s_mov_b32 %[cap], 0x10000\n\t" \
    "s_branch 2f\n\t" \
    "1:\n\t" \
    "s_bcnt1_i32_b32 %[t0], %[pend]\n\t" \
    "s_cmp_eq_u32 %[t0], 8\n\t" \
    "s_cbranch_scc0 48f\n\t" \
    "s_waitcnt vmcnt(16)\n\t" \
    "s_branch 40f\n\t" \
    "48:\n\t" \
    "s_cmp_eq_u32 %[t0], 7\n\t" \
    "s_cbranch_scc0 47f\n\t" \
    "s_waitcnt vmcnt(14)\n\t" \
    "s_branch 40f\n\t" \
    "47:\n\t" \
    "s_cmp_eq_u32 %[t0], 6\n\t" \
    "s_cbranch_scc0 46f\n\t" \
    "s_waitcnt vmcnt(12)\n\t" \
    "s_branch 40f\n\t" \
    "46:\n\t" \
    "s_cmp_eq_u32 %[t0], 5\n\t" \
    "s_cbranch_scc0 45f\n\t" \
    "s_waitcnt vmcnt(10)\n\t" \
    "s_branch 40f\n\t" \
    "45:\n\t" \
    "s_cmp_eq_u32 %[t0], 4\n\t" \
    "s_cbranch_scc0 44f\n\t" \
    "s_waitcnt vmcnt(8)\n\t" \
    "s_branch 40f\n\t" \
    "44:\n\t" \
    "s_cmp_eq_u32 %[t0], 3\n\t" \
    "s_cbranch_scc0 43f\n\t" \
    "s_waitcnt vmcnt(6)\n\t" \
    "s_branch 40f\n\t" \
    "43:\n\t" \
    "s_cmp_eq_u32 %[t0], 2\n\t" \
    "s_cbranch_scc0 42f\n\t" \
    "s_waitcnt vmcnt(4)\n\t" \
    "s_branch 40f\n\t" \
    "42:\n\t" \
    "s_cmp_eq_u32 %[t0], 1\n\t" \
    "s_cbranch_scc0 41f\n\t" \
    "s_waitcnt vmcnt(2)\n\t" \
    "s_branch 40f\n\t" \
    "41:\n\t" \
    "s_waitcnt vmcnt(0)\n\t" \
    "40:\n\t" \
    "s_andn2_b32 %[lnd], %[iss], %[pend]\n\t" \
    "v_cmp_eq_u64 vcc, %[tag], v[224:225]\n\t" \
    "s_sub_u32 %[cap], %[cap], 1\n\t" \
    "s_cbranch_scc1 3f\n\t" \
    "s_nop 1\n\t" \
    "s_and_b32 %[t0], vcc_lo, 0xff\n\t" \
    "2:\n\t" \
    "s_andn2_b32 %[pend], %[t0], %[iss]\n\t" \
    "s_or_b32 %[iss], %[iss], %[pend]\n\t" \
    "s_cmp_eq_u32 %[iss], 0xff\n\t" \
    "s_cbranch_scc1 4f\n\t" \
    "global_load_dwordx2 v[224:225], %[fl], off sc1\n\t" \
    "4:\n\t" \
    "s_bitcmp1_b32 %[pend], 0\n\t" \
    "s_cbranch_scc0 20f\n\t" \
    "s_add_u32 %[t0], %[so], 0\n\t" \
    "buffer_load_dwordx4 v[0:3], %[vl], %[srd], %[t0] offen sc1\n\t" \
    "buffer_load_dwordx4 v[4:7], %[vl], %[srd], %[t0] offen offset:1024 sc1\n\t" \
    "20:\n\t" \
    "s_bitcmp1_b32 %[pend], 1\n\t" \
    "s_cbranch_scc0 21f\n\t" \
    "s_add_u32 %[t0], %[so], 2048\n\t" \
    "buffer_load_dwordx4 v[8:11], %[vl], %[srd], %[t0] offen sc1\n\t" \
    "buffer_load_dwordx4 v[12:15], %[vl], %[srd], %[t0] offen offset:1024 sc1\n\t" \
    "21:\n\t" \
    "s_bitcmp1_b32 %[pend], 2\n\t" \
    "s_cbranch_scc0 22f\n\t" \
    "s_add_u32 %[t0], %[so], 4096\n\t" \
    "buffer_load_dwordx4 v[16:19], %[vl], %[srd], %[t0] offen sc1\n\t" \
    "buffer_load_dwordx4 v[20:23], %[vl], %[srd], %[t0] offen offset:1024 sc1\n\t" \
    "22:\n\t" \
    "s_bitcmp1_b32 %[pend], 3\n\t" \
    "s_cbranch_scc0 23f\n\t" \
    "s_add_u32 %[t0], %[so], 6144\n\t" \
    "buffer_load_dwordx4 v[24:27], %[vl], %[srd], %[t0] offen sc1\n\t" \
    "buffer_load_dwordx4 v[28:31], %[vl], %[srd], %[t0] offen offset:1024 sc1\n\t" \
    "23:\n\t" \
    "s_bitcmp1_b32 %[pend], 4\n\t" \
    "s_cbranch_scc0 24f\n\t" \
    "s_add_u32 %[t0], %[so], 8192\n\t" \
    "buffer_load_dwordx4 v[32:35], %[vl], %[srd], %[t0] offen sc1\n\t" \
    "buffer_load_dwordx4 v[36:39], %[vl], %[srd], %[t0] offen offset:1024 sc1\n\t" \
    "24:\n\t" \
    "s_bitcmp1_b32 %[pend], 5\n\t" \
    "s_cbranch_scc0 25f\n\t" \
    "s_add_u32 %[t0], %[so], 10240\n\t" \
    "buffer_load_dwordx4 v[40:43], %[vl], %[srd], %[t0] offen sc1\n\t" \
    "buffer_load_dwordx4 v[44:47], %[vl], %[srd], %[t0] offen offset:1024 sc1\n\t" \
    "25:\n\t" \
    "s_bitcmp1_b32 %[pend], 6\n\t" \
    "s_cbranch_scc0 26f\n\t" \
    "s_add_u32 %[t0], %[so], 12288\n\t" \
    "buffer_load_dwordx4 v[48:51], %[vl], %[srd], %[t0] offen sc1\n\t" \
    "buffer_load_dwordx4 v[52:55], %[vl], %[srd], %[t0] offen offset:1024 sc1\n\t" \
    "26:\n\t" \
    "s_bitcmp1_b32 %[pend], 7\n\t" \
    "s_cbranch_scc0 27f\n\t" \
    "s_add_u32 %[t0], %[so], 14336\n\t" \
    "buffer_load_dwordx4 v[56:59], %[vl], %[srd], %[t0] offen sc1\n\t" \
    "buffer_load_dwordx4 v[60:63], %[vl], %[srd], %[t0] offen offset:1024 sc1\n\t" \
    "27:\n\t" \
    "s_cmp_lg_u32 %[nxt], 0\n\t" \
    "s_cbranch_scc1 10f\n\t" \
    "s_bitcmp1_b32 %[lnd], 0\n\t" \
    "s_cbranch_scc0 10f\n\t" \
    "v_mfma_f32_32x32x16_bf16 v[192:207], v[64:67], v[0:3], v[192:207]\n\t" \
    "v_mfma_f32_32x32x16_bf16 v[208:223], v[68:71], v[0:3], v[208:223]\n\t" \
    "v_mfma_f32_32x32x16_bf16 v[192:207], v[72:75], v[4:7], v[192:207]\n\t" \
    "v_mfma_f32_32x32x16_bf16 v[208:223], v[76:79], v[4:7], v[208:223]\n\t" \
    "s_mov_b32 %[nxt], 1\n\t" \
    "10:\n\t" \
    "s_cmp_lg_u32 %[nxt], 1\n\t" \
    "s_cbranch_scc1 11f\n\t" \
    "s_bitcmp1_b32 %[lnd], 1\n\t" \
    "s_cbranch_scc0 11f\n\t" \
    "v_mfma_f32_32x32x16_bf16 v[192:207], v[80:83], v[8:11], v[192:207]\n\t" \
    "v_mfma_f32_32x32x16_bf16 v[208:223], v[84:87], v[8:11], v[208:223]\n\t" \
    "v_mfma_f32_32x32x16_bf16 v[192:207], v[88:91], v[12:15], v[192:207]\n\t" \
    "v_mfma_f32_32x32x16_bf16 v[208:223], v[92:95], v[12:15], v[208:223]\n\t" \
    "s_mov_b32 %[nxt], 2\n\t" \
    "11:\n\t" \
    "s_cmp_lg_u32 %[nxt], 2\n\t" \
    "s_cbranch_scc1 12f\n\t" \
    "s_bitcmp1_b32 %[lnd], 2\n\t" \
    "s_cbranch_scc0 12f\n\t" \
    "v_mfma_f32_32x32x16_bf16 v[192:207], v[96:99], v[16:19], v[192:207]\n\t" \
    "v_mfma_f32_32x32x16_bf16 v[208:223], v[100:103], v[16:19], v[208:223]\n\t" \
    "v_mfma_f32_32x32x16_bf16 v[192:207], v[104:107], v[20:23], v[192:207]\n\t" \
    "v_mfma_f32_32x32x16_bf16 v[208:223], v[108:111], v[20:23], v[208:223]\n\t" \
    "s_mov_b32 %[nxt], 3\n\t" \
    "12:\n\t" \
    "s_cmp_lg_u32 %[nxt], 3\n\t" \
    "s_cbranch_scc1 13f\n\t" \
    "s_bitcmp1_b32 %[lnd], 3\n\t" \
    "s_cbranch_scc0 13f\n\t" \
    "v_mfma_f32_32x32x16_bf16 v[192:207], v[112:115], v[24:27], v[192:207]\n\t" \
    "v_mfma_f32_32x32x16_bf16 v[208:223], v[116:119], v[24:27], v[208:223]\n\t" \
    "v_mfma_f32_32x32x16_bf16 v[192:207], v[120:123], v[28:31], v[192:207]\n\t" \
    "v_mfma_f32_32x32x16_bf16 v[208:223], v[124:127], v[28:31], v[208:223]\n\t" \
    "s_mov_b32 %[nxt], 4\n\t" \
    "13:\n\t" \
    "s_cmp_lg_u32 %[nxt], 4\n\t" \
    "s_cbranch_scc1 14f\n\t" \
    "s_bitcmp1_b32 %[lnd], 4\n\t" \
    "s_cbranch_scc0 14f\n\t" \
    "v_mfma_f32_32x32x16_bf16 v[192:207], v[128:131], v[32:35], v[192:207]\n\t" \
    "v_mfma_f32_32x32x16_bf16 v[208:223], v[132:135], v[32:35], v[208:223]\n\t" \
    "v_mfma_f32_32x32x16_bf16 v[192:207], v[136:139], v[36:39], v[192:207]\n\t" \
    "v_mfma_f32_32x32x16_bf16 v[208:223], v[140:143], v[36:39], v[208:223]\n\t" \
    "s_mov_b32 %[nxt], 5\n\t" \
    "14:\n\t" \
    "s_cmp_lg_u32 %[nxt], 5\n\t" \
    "s_cbranch_scc1 15f\n\t" \
    "s_bitcmp1_b32 %[lnd], 5\n\t" \
    "s_cbranch_scc0 15f\n\t" \
    "v_mfma_f32_32x32x16_bf16 v[192:207], v[144:147], v[40:43], v[192:207]\n\t" \
    "v_mfma_f32_32x32x16_bf16 v[208:223], v[148:151], v[40:43], v[208:223]\n\t" \
    "v_mfma_f32_32x32x16_bf16 v[192:207], v[152:155], v[44:47], v[192:207]\n\t" \
    "v_mfma_f32_32x32x16_bf16 v[208:223], v[156:159], v[44:47], v[208:223]\n\t" \
    "s_mov_b32 %[nxt], 6\n\t" \
    "15:\n\t" \
    "s_cmp_lg_u32 %[nxt], 6\n\t" \
    "s_cbranch_scc1 16f\n\t" \
    "s_bitcmp1_b32 %[lnd], 6\n\t" \
    "s_cbranch_scc0 16f\n\t" \
    "v_mfma_f32_32x32x16_bf16 v[192:207], v[160:163], v[48:51], v[192:207]\n\t" \
    "v_mfma_f32_32x32x16_bf16 v[208:223], v[164:167], v[48:51], v[208:223]\n\t" \
    "v_mfma_f32_32x32x16_bf16 v[192:207], v[168:171], v[52:55], v[192:207]\n\t" \
    "v_mfma_f32_32x32x16_bf16 v[208:223], v[172:175], v[52:55], v[208:223]\n\t" \
    "s_mov_b32 %[nxt], 7\n\t" \
    "16:\n\t" \
    "s_cmp_lg_u32 %[nxt], 7\n\t" \
    "s_cbranch_scc1 17f\n\t" \
    "s_bitcmp1_b32 %[lnd], 7\n\t" \
    "s_cbranch_scc0 17f\n\t" \
    "v_mfma_f32_32x32x16_bf16 v[192:207], v[176:179], v[56:59], v[192:207]\n\t" \
    "v_mfma_f32_32x32x16_bf16 v[208:223], v[180:183], v[56:59], v[208:223]\n\t" \
    "v_mfma_f32_32x32x16_bf16 v[192:207], v[184:187], v[60:63], v[192:207]\n\t" \
    "v_mfma_f32_32x32x16_bf16 v[208:223], v[188:191], v[60:63], v[208:223]\n\t" \
    "s_mov_b32 %[nxt], 8\n\t" \
    "17:\n\t" \
    "s_cmp_eq_u32 %[iss], 0xff\n\t" \
    "s_cbranch_scc0 1b\n\t" \
    "s_waitcnt vmcnt(0)\n\t" \
    "s_mov_b32 %[lnd], 0xff\n\t" \
    "s_cmp_lg_u32 %[nxt], 0\n\t" \
    "s_cbranch_scc1 50f\n\t" \
    "s_bitcmp1_b32 %[lnd], 0\n\t" \
    "s_cbranch_scc0 50f\n\t" \
    "v_mfma_f32_32x32x16_bf16 v[192:207], v[64:67], v[0:3], v[192:207]\n\t" \
    "v_mfma_f32_32x32x16_bf16 v[208:223], v[68:71], v[0:3], v[208:223]\n\t" \
    "v_mfma_f32_32x32x16_bf16 v[192:207], v[72:75], v[4:7], v[192:207]\n\t" \
    "v_mfma_f32_32x32x16_bf16 v[208:223], v[76:79], v[4:7], v[208:223]\n\t" \
    "s_mov_b32 %[nxt], 1\n\t" \
    "50:\n\t" \
    "s_cmp_lg_u32 %[nxt], 1\n\t" \
    "s_cbranch_scc1 51f\n\t" \
    "s_bitcmp1_b32 %[lnd], 1\n\t" \
    "s_cbranch_scc0 51f\n\t" \
    "v_mfma_f32_32x32x16_bf16 v[192:207], v[80:83], v[8:11], v[192:207]\n\t" \
    "v_mfma_f32_32x32x16_bf16 v[208:223], v[84:87], v[8:11], v[208:223]\n\t" \
    "v_mfma_f32_32x32x16_bf16 v[192:207], v[88:91], v[12:15], v[192:207]\n\t" \
    "v_mfma_f32_32x32x16_bf16 v[208:223], v[92:95], v[12:15], v[208:223]\n\t" \
    "s_mov_b32 %[nxt], 2\n\t" \
    "51:\n\t" \
    "s_cmp_lg_u32 %[nxt], 2\n\t" \
    "s_cbranch_scc1 52f\n\t" \
    "s_bitcmp1_b32 %[lnd], 2\n\t" \
    "s_cbranch_scc0 52f\n\t" \
    "v_mfma_f32_32x32x16_bf16 v[192:207], v[96:99], v[16:19], v[192:207]\n\t" \
    "v_mfma_f32_32x32x16_bf16 v[208:223], v[100:103], v[16:19], v[208:223]\n\t" \
    "v_mfma_f32_32x32x16_bf16 v[192:207], v[104:107], v[20:23], v[192:207]\n\t" \
    "v_mfma_f32_32x32x16_bf16 v[208:223], v[108:111], v[20:23], v[208:223]\n\t" \
    "s_mov_b32 %[nxt], 3\n\t" \
    "52:\n\t" \
    "s_cmp_lg_u32 %[nxt], 3\n\t" \
    "s_cbranch_scc1 53f\n\t" \
    "s_bitcmp1_b32 %[lnd], 3\n\t" \
    "s_cbranch_scc0 53f\n\t" \
    "v_mfma_f32_32x32x16_bf16 v[192:207], v[112:115], v[24:27], v[192:207]\n\t" \
    "v_mfma_f32_32x32x16_bf16 v[208:223], v[116:119], v[24:27], v[208:223]\n\t" \
    "v_mfma_f32_32x32x16_bf16 v[192:207], v[120:123], v[28:31], v[192:207]\n\t" \
    "v_mfma_f32_32x32x16_bf16 v[208:223], v[124:127], v[28:31], v[208:223]\n\t" \
    "s_mov_b32 %[nxt], 4\n\t" \
    "53:\n\t" \
    "s_cmp_lg_u32 %[nxt], 4\n\t" \
    "s_cbranch_scc1 54f\n\t" \
    "s_bitcmp1_b32 %[lnd], 4\n\t" \
    "s_cbranch_scc0 54f\n\t" \
    "v_mfma_f32_32x32x16_bf16 v[192:207], v[128:131], v[32:35], v[192:207]\n\t" \
    "v_mfma_f32_32x32x16_bf16 v[208:223], v[132:135], v[32:35], v[208:223]\n\t" \
    "v_mfma_f32_32x32x16_bf16 v[192:207], v[136:139], v[36:39], v[192:207]\n\t" \
    "v_mfma_f32_32x32x16_bf16 v[208:223], v[140:143], v[36:39], v[208:223]\n\t" \
    "s_mov_b32 %[nxt], 5\n\t" \
    "54:\n\t" \
    "s_cmp_lg_u32 %[nxt], 5\n\t" \
    "s_cbranch_scc1 55f\n\t" \
    "s_bitcmp1_b32 %[lnd], 5\n\t" \
    "s_cbranch_scc0 55f\n\t" \
    "v_mfma_f32_32x32x16_bf16 v[192:207], v[144:147], v[40:43], v[192:207]\n\t" \
    "v_mfma_f32_32x32x16_bf16 v[208:223], v[148:151], v[40:43], v[208:223]\n\t" \
    "v_mfma_f32_32x32x16_bf16 v[192:207], v[152:155], v[44:47], v[192:207]\n\t" \
    "v_mfma_f32_32x32x16_bf16 v[208:223], v[156:159], v[44:47], v[208:223]\n\t" \
    "s_mov_b32 %[nxt], 6\n\t" \
    "55:\n\t" \
    "s_cmp_lg_u32 %[nxt], 6\n\t" \
    "s_cbranch_scc1 56f\n\t" \
    "s_bitcmp1_b32 %[lnd], 6\n\t" \
    "s_cbranch_scc0 56f\n\t" \
    "v_mfma_f32_32x32x16_bf16 v[192:207], v[160:163], v[48:51], v[192:207]\n\t" \
    "v_mfma_f32_32x32x16_bf16 v[208:223], v[164:167], v[48:51], v[208:223]\n\t" \
    "v_mfma_f32_32x32x16_bf16 v[192:207], v[168:171], v[52:55], v[192:207]\n\t" \
    "v_mfma_f32_32x32x16_bf16 v[208:223], v[172:175], v[52:55], v[208:223]\n\t" \
    "s_mov_b32 %[nxt], 7\n\t" \
    "56:\n\t" \
    "s_cmp_lg_u32 %[nxt], 7\n\t" \
    "s_cbranch_scc1 57f\n\t" \
    "s_bitcmp1_b32 %[lnd], 7\n\t" \
    "s_cbranch_scc0 57f\n\t" \
    "v_mfma_f32_32x32x16_bf16 v[192:207], v[176:179], v[56:59], v[192:207]\n\t" \
    "v_mfma_f32_32x32x16_bf16 v[208:223], v[180:183], v[56:59], v[208:223]\n\t" \
    "v_mfma_f32_32x32x16_bf16 v[192:207], v[184:187], v[60:63], v[192:207]\n\t" \
    "v_mfma_f32_32x32x16_bf16 v[208:223], v[188:191], v[60:63], v[208:223]\n\t" \
    "s_mov_b32 %[nxt], 8\n\t" \
    "57:\n\t" \
    "3:\n\t" \
    "s_nop 7\n\t" \
    "s_nop 7\n\t" \
    "s_mov_b32 %[cap], 0x10000\n\t" \
    "74:\n\t" \
    "ds_read_b32 v225, %[rd]\n\t" \
    "s_waitcnt lgkmcnt(0)\n\t" \
    "v_readfirstlane_b32 %[t0], v225\n\t" \
    "s_sub_u32 %[cap], %[cap], 1\n\t" \
    "s_cbranch_scc1 75f\n\t" \
    "s_cmp_lt_u32 %[t0], 1\n\t" \
    "s_cbranch_scc1 74b\n\t" \
    "75:\n\t" \
    "s_cmp_eq_u32 %[kq], 0\n\t" \
    "s_cbranch_scc1 64f\n\t" \
    "ds_write_b128 %[sm], v[192:195] offset:0\n\t" \
    "ds_write_b128 %[sm], v[196:199] offset:1024\n\t" \
    "ds_write_b128 %[sm], v[200:203] offset:2048\n\t" \
    "ds_write_b128 %[sm], v[204:207] offset:3072\n\t" \
    "v_mov_b64 v[192:193], v[208:209]\n\t" \
    "v_mov_b64 v[194:195], v[210:211]\n\t" \
    "v_mov_b64 v[196:197], v[212:213]\n\t" \
    "v_mov_b64 v[198:199], v[214:215]\n\t" \
    "v_mov_b64 v[200:201], v[216:217]\n\t" \
    "v_mov_b64 v[202:203], v[218:219]\n\t" \
    "v_mov_b64 v[204:205], v[220:221]\n\t" \
    "v_mov_b64 v[206:207], v[222:223]\n\t" \
    "s_branch 65f\n\t" \
    "64:\n\t" \
    "ds_write_b128 %[sm], v[208:211] offset:0\n\t" \
    "ds_write_b128 %[sm], v[212:215] offset:1024\n\t" \
    "ds_write_b128 %[sm], v[216:219] offset:2048\n\t" \
    "ds_write_b128 %[sm], v[220:223] offset:3072\n\t" \
    "65:\n\t" \
    "v_mov_b32 v224, 1\n\t" \
    "s_waitcnt lgkmcnt(0)\n\t" \
    "s_mov_b64 exec, 1\n\t" \
    "ds_write_b32 %[fm], v224\n\t" \
    "s_mov_b64 exec, -1\n\t" \
    "s_mov_b32 %[cap], 0x10000\n\t" \
    "66:\n\t" \
    "ds_read_b32 v225, %[fp]\n\t" \
    "s_waitcnt lgkmcnt(0)\n\t" \
    "v_readfirstlane_b32 %[t0], v225\n\t" \
    "s_sub_u32 %[cap], %[cap], 1\n\t" \
    "s_cbranch_scc1 67f\n\t" \
    "s_cmp_lt_u32 %[t0], 1\n\t" \
    "s_cbranch_scc1 66b\n\t" \
    "67:\n\t" \
    "ds_read_b128 v[0:3], %[sp] offset:0\n\t" \
    "ds_read_b128 v[4:7], %[sp] offset:1024\n\t" \
    "ds_read_b128 v[8:11], %[sp] offset:2048\n\t" \
    "ds_read_b128 v[12:15], %[sp] offset:3072\n\t" \
    "s_waitcnt lgkmcnt(0)\n\t" \
    "v_pk_add_f32 v[192:193], v[192:193], v[0:1]\n\t" \
    "v_pk_add_f32 v[194:195], v[194:195], v[2:3]\n\t" \
    "v_pk_add_f32 v[196:197], v[196:197], v[4:5]\n\t" \
    "v_pk_add_f32 v[198:199], v[198:199], v[6:7]\n\t" \
    "v_pk_add_f32 v[200:201], v[200:201], v[8:9]\n\t" \
    "v_pk_add_f32 v[202:203], v[202:203], v[10:11]\n\t" \
    "v_pk_add_f32 v[204:205], v[204:205], v[12:13]\n\t" \
    "v_pk_add_f32 v[206:207], v[206:207], v[14:15]\n\t" \
    "v_cvt_pk_bf16_f32 v0, v192, v193\n\t" \
    "v_cvt_pk_bf16_f32 v1, v194, v195\n\t" \
    "v_cvt_pk_bf16_f32 v2, v196, v197\n\t" \
    "v_cvt_pk_bf16_f32 v3, v198, v199\n\t" \
    "v_cvt_pk_bf16_f32 v4, v200, v201\n\t" \
    "v_cvt_pk_bf16_f32 v5, v202, v203\n\t" \
    "v_cvt_pk_bf16_f32 v6, v204, v205\n\t" \
    "v_cvt_pk_bf16_f32 v7, v206, v207\n\t" \
    "s_nop 1\n\t" \
    "v_permlane32_swap_b32 v0, v2\n\t" \
    "v_permlane32_swap_b32 v1, v3\n\t" \
    "v_permlane32_swap_b32 v4, v6\n\t" \
    "v_permlane32_swap_b32 v5, v7\n\t" \
    "global_store_dwordx4 %[out], v[0:3], off\n\t" \
    "global_store_dwordx4 %[out], v[4:7], off offset:32\n\t" \
    "s_nop 1\n\t" \
    ""
#define ATTN_A_PROBE \
    "ds_read_b128 v[48:51], %[qa] offset:0\n\t" \
    "ds_read_b128 v[52:55], %[qa] offset:1024\n\t" \
    "ds_read_b128 v[144:147], %[qa] offset:2048\n\t" \
    "ds_read_b128 v[148:151], %[qa] offset:3072\n\t" \
    "ds_read_b128 v[184:187], %[pp] offset:0\n\t" \
    "ds_read_b128 v[188:191], %[pp] offset:1024\n\t" \
    "s_cmp_eq_u32 %[kh], 0\n\t" \
    "s_cbranch_scc1 1f\n\t" \
    "s_waitcnt lgkmcnt(1)\n\t" \
    "v_mfma_f32_32x32x16_bf16 v[0:15], v[184:187], v[48:51], v[168:183]\n\t" \
    "s_branch 2f\n\t" \
    "1:\n\t" \
    "s_waitcnt lgkmcnt(1)\n\t" \
    "v_mfma_f32_32x32x16_bf16 v[0:15], v[184:187], v[48:51], 0\n\t" \
    "2:\n\t" \
    "s_waitcnt lgkmcnt(0)\n\t" \
    "v_mfma_f32_32x32x16_bf16 v[0:15], v[188:191], v[52:55], v[0:15]\n\t" \
    "v_mfma_f32_32x32x16_bf16 v[96:111], v[184:187], v[144:147], 0\n\t" \
    "v_mfma_f32_32x32x16_bf16 v[96:111], v[188:191], v[148:151], v[96:111]\n\t" \
    "s_nop 7\n\t" \
    "v_max3_f32 v192, v0, v1, v2\n\t" \
    "v_max3_f32 v193, v3, v4, v5\n\t" \
    "v_max3_f32 v193, v6, v7, v193\n\t" \
    "v_max3_f32 v192, v8, v9, v192\n\t" \
    "v_max3_f32 v193, v10, v11, v193\n\t" \
    "v_max3_f32 v192, v12, v13, v192\n\t" \
    "v_max3_f32 v193, v14, v15, v193\n\t" \
    "v_max_f32 v192, v192, v193\n\t" \
    "v_mov_b32 v196, v192\n\t" \
    "s_nop 3\n\t" \
    "v_max3_f32 v194, v96, v97, v98\n\t" \
    "v_max3_f32 v195, v99, v100, v101\n\t" \
    "v_max3_f32 v195, v102, v103, v195\n\t" \
    "v_max3_f32 v194, v104, v105, v194\n\t" \
    "v_max3_f32 v195, v106, v107, v195\n\t" \
    "v_max3_f32 v194, v108, v109, v194\n\t" \
    "v_max3_f32 v195, v110, v111, v195\n\t" \
    "v_permlane32_swap_b32 v192, v196\n\t" \
    "v_max_f32 v194, v194, v195\n\t" \
    "v_mov_b32 v197, v194\n\t" \
    "v_max_f32 v192, v192, v196\n\t" \
    "v_add_f32 v192, %[bias], v192\n\t" \
    "v_permlane32_swap_b32 v194, v197\n\t" \
    "v_cvt_pk_bf16_f32 v192, -v192, 0\n\t" \
    "v_max_f32 v194, v194, v197\n\t" \
    "v_add_f32 v194, %[bias], v194\n\t" \
    "v_and_b32 v192, 0xffff, v192\n\t" \
    "v_cvt_pk_bf16_f32 v194, -v194, 0\n\t" \
    "v_cndmask_b32_e64 v54, v54, v192, %[hi]\n\t" \
    "v_and_b32 v194, 0xffff, v194\n\t" \
    "v_lshlrev_b32 v192, 16, v192\n\t" \
    "v_cndmask_b32_e64 v150, v150, v194, %[hi]\n\t" \
    "v_lshlrev_b32 v194, 16, v194\n\t" \
    "v_sub_f32 %[mX], 0, v192\n\t" \
    "v_sub_f32 %[mY], 0, v194\n\t" \
    ""
#define ATTN_B_PRO \
    "ds_read_b128 v[48:51], %[qa] offset:0\n\t" \
    "ds_read_b128 v[52:55], %[qa] offset:1024\n\t" \
    "ds_read_b128 v[144:147], %[qa] offset:2048\n\t" \
    "ds_read_b128 v[148:151], %[qa] offset:3072\n\t" \
    "ds_read_b128 v[184:187], v80 offset:0\n\t" \
    "ds_read_b128 v[188:191], v80 offset:1024\n\t" \
    "ds_read_b128 v[56:59], v80 offset:4096\n\t" \
    "ds_read_b128 v[60:63], v80 offset:5120\n\t" \
    "s_waitcnt lgkmcnt(3)\n\t" \
    "v_mfma_f32_32x32x16_bf16 v[0:15], v[184:187], v[48:51], 0\n\t" \
    "s_waitcnt lgkmcnt(2)\n\t" \
    "v_mfma_f32_32x32x16_bf16 v[0:15], v[188:191], v[52:55], v[0:15]\n\t" \
    "v_mfma_f32_32x32x16_bf16 v[96:111], v[184:187], v[144:147], 0\n\t" \
    "v_mfma_f32_32x32x16_bf16 v[96:111], v[188:191], v[148:151], v[96:111]\n\t" \
    "s_waitcnt lgkmcnt(0)\n\t" \
    "v_mfma_f32_32x32x16_bf16 v[16:31], v[56:59], v[48:51], 0\n\t" \
    "v_mfma_f32_32x32x16_bf16 v[16:31], v[60:63], v[52:55], v[16:31]\n\t" \
    "v_mfma_f32_32x32x16_bf16 v[112:127], v[56:59], v[144:147], 0\n\t" \
    "v_max3_f32 v192, v0, v1, v2\n\t" \
    "v_max3_f32 v193, v3, v4, v5\n\t" \
    "v_max3_f32 v193, v6, v7, v193\n\t" \
    "v_max3_f32 v192, v8, v9, v192\n\t" \
    "v_max3_f32 v193, v10, v11, v193\n\t" \
    "v_max3_f32 v192, v12, v13, v192\n\t" \
    "v_max3_f32 v193, v14, v15, v193\n\t" \
    "v_mfma_f32_32x32x16_bf16 v[112:127], v[60:63], v[148:151], v[112:127]\n\t" \
    "v_max3_f32 v194, v96, v97, v98\n\t" \
    "v_max3_f32 v195, v99, v100, v101\n\t" \
    "v_max3_f32 v195, v102, v103, v195\n\t" \
    "v_max3_f32 v194, v104, v105, v194\n\t" \
    "v_max3_f32 v195, v106, v107, v195\n\t" \
    "v_max3_f32 v194, v108, v109, v194\n\t" \
    "v_max3_f32 v195, v110, v111, v195\n\t" \
    "v_max3_f32 v192, v16, v17, v192\n\t" \
    "v_max3_f32 v193, v18, v19, v193\n\t" \
    "v_max3_f32 v192, v20, v21, v192\n\t" \
    "v_max3_f32 v193, v22, v23, v193\n\t" \
    "v_max3_f32 v192, v24, v25, v192\n\t" \
    "v_max3_f32 v193, v26, v27, v193\n\t" \
    "v_max3_f32 v192, v28, v29, v192\n\t" \
    "v_max3_f32 v193, v30, v31, v193\n\t" \
    "v_max_f32 v192, v192, v193\n\t" \
    "v_mov_b32 v196, v192\n\t" \
    "s_nop 7\n\t" \
    "v_permlane32_swap_b32 v192, v196\n\t" \
    "v_max3_f32 v194, v112, v113, v194\n\t" \
    "v_max3_f32 v195, v114, v115, v195\n\t" \
    "v_max3_f32 v194, v116, v117, v194\n\t" \
    "v_max3_f32 v195, v118, v119, v195\n\t" \
    "v_max3_f32 v194, v120, v121, v194\n\t" \
    "v_max3_f32 v195, v122, v123, v195\n\t" \
    "v_max3_f32 v194, v124, v125, v194\n\t" \
    "v_max3_f32 v195, v126, v127, v195\n\t" \
    "v_max_f32 v194, v194, v195\n\t" \
    "v_mov_b32 v197, v194\n\t" \
    "v_max_f32 v192, v192, v196\n\t" \
    "v_add_f32 v192, %[bias], v192\n\t" \
    "v_permlane32_swap_b32 v194, v197\n\t" \
    "v_cvt_pk_bf16_f32 v192, -v192, 0\n\t" \
    "v_max_f32 v194, v194, v197\n\t" \
    "v_add_f32 v194, %[bias], v194\n\t" \
    "v_and_b32 v192, 0xffff, v192\n\t" \
    "v_cvt_pk_bf16_f32 v194, -v194, 0\n\t" \
    "v_cndmask_b32_e64 v54, v54, v192, %[hi]\n\t" \
    "v_and_b32 v194, 0xffff, v194\n\t" \
    "v_lshlrev_b32 v192, 16, v192\n\t" \
    "v_cndmask_b32_e64 v150, v150, v194, %[hi]\n\t" \
    "v_lshlrev_b32 v194, 16, v194\n\t" \
    "v_pk_add_f32 v[0:1], v[0:1], v[192:193] op_sel_hi:[1,0]\n\t" \
    "v_pk_add_f32 v[2:3], v[2:3], v[192:193] op_sel_hi:[1,0]\n\t" \
    "v_pk_add_f32 v[4:5], v[4:5], v[192:193] op_sel_hi:[1,0]\n\t" \
    "v_pk_add_f32 v[6:7], v[6:7], v[192:193] op_sel_hi:[1,0]\n\t" \
    "v_pk_add_f32 v[8:9], v[8:9], v[192:193] op_sel_hi:[1,0]\n\t" \
    "v_pk_add_f32 v[10:11], v[10:11], v[192:193] op_sel_hi:[1,0]\n\t" \
    "v_pk_add_f32 v[12:13], v[12:13], v[192:193] op_sel_hi:[1,0]\n\t" \
    "v_pk_add_f32 v[14:15], v[14:15], v[192:193] op_sel_hi:[1,0]\n\t" \
    "v_pk_add_f32 v[96:97], v[96:97], v[194:195] op_sel_hi:[1,0]\n\t" \
    "v_pk_add_f32 v[98:99], v[98:99], v[194:195] op_sel_hi:[1,0]\n\t" \
    "v_pk_add_f32 v[100:101], v[100:101], v[194:195] op_sel_hi:[1,0]\n\t" \
    "v_pk_add_f32 v[102:103], v[102:103], v[194:195] op_sel_hi:[1,0]\n\t" \
    "v_pk_add_f32 v[104:105], v[104:105], v[194:195] op_sel_hi:[1,0]\n\t" \
    "v_pk_add_f32 v[106:107], v[106:107], v[194:195] op_sel_hi:[1,0]\n\t" \
    "v_pk_add_f32 v[108:109], v[108:109], v[194:195] op_sel_hi:[1,0]\n\t" \
    "v_pk_add_f32 v[110:111], v[110:111], v[194:195] op_sel_hi:[1,0]\n\t" \
    "v_sub_f32 %[mX], 0, v192\n\t" \
    "v_sub_f32 %[mY], 0, v194\n\t" \
    ""
#define FINISH_X \
    "ds_read_b32 v16, %[pm]\n\t" \
    "ds_read_b128 v[0:3], %[pa] offset:0\n\t" \
    "ds_read_b128 v[4:7], %[pa] offset:1024\n\t" \
    "ds_read_b128 v[8:11], %[pa] offset:2048\n\t" \
    "ds_read_b128 v[12:15], %[pa] offset:3072\n\t" \
    "v_mov_b32 v27, 0x207\n\t" \
    "s_waitcnt lgkmcnt(4)\n\t" \
    "v_max_f32 v17, %[m], v16\n\t" \
    "v_sub_f32 v18, %[m], v17\n\t" \
    "v_sub_f32 v19, v16, v17\n\t" \
    "v_exp_f32 v18, v18\n\t" \
    "v_exp_f32 v19, v19\n\t" \
    "s_waitcnt lgkmcnt(0)\n\t" \
    "v_mul_f32 v20, v12, v19\n\t" \
    "v_fma_f32 v20, v44, v18, v20\n\t" \
    "v_mov_b32 v21, v20\n\t" \
    "s_mov_b64 exec, 1\n\t" \
    "ds_write_b32 %[rd], %[ep]\n\t" \
    "s_mov_b64 exec, -1\n\t" \
    "v_permlane32_swap_b32 v20, v21\n\t" \
    "v_rcp_f32 v22, v21\n\t" \
    "v_cmp_class_f32 vcc, v21, v27\n\t" \
    "v_mul_f32 v24, v18, v22\n\t" \
    "v_mul_f32 v26, v19, v22\n\t" \
    "s_mov_b64 %[bad], vcc\n\t" \
    "v_cmp_gt_f32 vcc, %[lmin], v21\n\t" \
    "v_pk_mul_f32 v[0:1], v[0:1], v[26:27] op_sel_hi:[1,0]\n\t" \
    "v_pk_mul_f32 v[2:3], v[2:3], v[26:27] op_sel_hi:[1,0]\n\t" \
    "v_pk_mul_f32 v[4:5], v[4:5], v[26:27] op_sel_hi:[1,0]\n\t" \
    "v_pk_mul_f32 v[6:7], v[6:7], v[26:27] op_sel_hi:[1,0]\n\t" \
    "v_pk_mul_f32 v[8:9], v[8:9], v[26:27] op_sel_hi:[1,0]\n\t" \
    "v_pk_mul_f32 v[10:11], v[10:11], v[26:27] op_sel_hi:[1,0]\n\t" \
    "v_pk_mul_f32 v[12:13], v[12:13], v[26:27] op_sel_hi:[1,0]\n\t" \
    "v_pk_mul_f32 v[14:15], v[14:15], v[26:27] op_sel_hi:[1,0]\n\t" \
    "s_or_b64 %[bad], %[bad], vcc\n\t" \
    "v_pk_fma_f32 v[0:1], v[32:33], v[24:25], v[0:1] op_sel_hi:[1,0,1]\n\t" \
    "v_pk_fma_f32 v[2:3], v[34:35], v[24:25], v[2:3] op_sel_hi:[1,0,1]\n\t" \
    "v_pk_fma_f32 v[4:5], v[36:37], v[24:25], v[4:5] op_sel_hi:[1,0,1]\n\t" \
    "v_pk_fma_f32 v[6:7], v[38:39], v[24:25], v[6:7] op_sel_hi:[1,0,1]\n\t" \
    "v_pk_fma_f32 v[8:9], v[40:41], v[24:25], v[8:9] op_sel_hi:[1,0,1]\n\t" \
    "v_pk_fma_f32 v[10:11], v[42:43], v[24:25], v[10:11] op_sel_hi:[1,0,1]\n\t" \
    "v_pk_fma_f32 v[12:13], v[44:45], v[24:25], v[12:13] op_sel_hi:[1,0,1]\n\t" \
    "v_pk_fma_f32 v[14:15], v[46:47], v[24:25], v[14:15] op_sel_hi:[1,0,1]\n\t" \
    "v_pk_add_f32 v[16:17], v[0:1], v[2:3]\n\t" \
    "v_pk_add_f32 v[18:19], v[4:5], v[6:7]\n\t" \
    "v_pk_add_f32 v[20:21], v[8:9], v[10:11]\n\t" \
    "v_pk_add_f32 v[22:23], v[12:13], v[14:15]\n\t" \
    "v_pk_add_f32 v[16:17], v[16:17], v[18:19]\n\t" \
    "v_pk_add_f32 v[20:21], v[20:21], v[22:23]\n\t" \
    "v_mov_b32 v27, 0x207\n\t" \
    "v_pk_add_f32 v[16:17], v[16:17], v[20:21]\n\t" \
    "v_add_f32 v16, v16, v17\n\t" \
    "v_cmp_class_f32 vcc, v16, v27\n\t" \
    "s_nop 1\n\t" \
    "s_or_b64 %[bad], %[bad], vcc\n\t" \
    ""
#define FINISH_Y \
    "ds_read_b32 v16, %[pm]\n\t" \
    "ds_read_b128 v[0:3], %[pa] offset:0\n\t" \
    "ds_read_b128 v[4:7], %[pa] offset:1024\n\t" \
    "ds_read_b128 v[8:11], %[pa] offset:2048\n\t" \
    "ds_read_b128 v[12:15], %[pa] offset:3072\n\t" \
    "v_mov_b32 v27, 0x207\n\t" \
    "s_waitcnt lgkmcnt(4)\n\t" \
    "v_max_f32 v17, %[m], v16\n\t" \
    "v_sub_f32 v18, %[m], v17\n\t" \
    "v_sub_f32 v19, v16, v17\n\t" \
    "v_exp_f32 v18, v18\n\t" \
    "v_exp_f32 v19, v19\n\t" \
    "s_waitcnt lgkmcnt(0)\n\t" \
    "v_mul_f32 v20, v12, v19\n\t" \
    "v_fma_f32 v20, v140, v18, v20\n\t" \
    "v_mov_b32 v21, v20\n\t" \
    "s_mov_b64 exec, 1\n\t" \
    "ds_write_b32 %[rd], %[ep]\n\t" \
    "s_mov_b64 exec, -1\n\t" \
    "v_permlane32_swap_b32 v20, v21\n\t" \
    "v_rcp_f32 v22, v21\n\t" \
    "v_cmp_class_f32 vcc, v21, v27\n\t" \
    "v_mul_f32 v24, v18, v22\n\t" \
    "v_mul_f32 v26, v19, v22\n\t" \
    "s_mov_b64 %[bad], vcc\n\t" \
    "v_cmp_gt_f32 vcc, %[lmin], v21\n\t" \
    "v_pk_mul_f32 v[0:1], v[0:1], v[26:27] op_sel_hi:[1,0]\n\t" \
    "v_pk_mul_f32 v[2:3], v[2:3], v[26:27] op_sel_hi:[1,0]\n\t" \
    "v_pk_mul_f32 v[4:5], v[4:5], v[26:27] op_sel_hi:[1,0]\n\t" \
    "v_pk_mul_f32 v[6:7], v[6:7], v[26:27] op_sel_hi:[1,0]\n\t" \
    "v_pk_mul_f32 v[8:9], v[8:9], v[26:27] op_sel_hi:[1,0]\n\t" \
    "v_pk_mul_f32 v[10:11], v[10:11], v[26:27] op_sel_hi:[1,0]\n\t" \
    "v_pk_mul_f32 v[12:13], v[12:13], v[26:27] op_sel_hi:[1,0]\n\t" \
    "v_pk_mul_f32 v[14:15], v[14:15], v[26:27] op_sel_hi:[1,0]\n\t" \
    "s_or_b64 %[bad], %[bad], vcc\n\t" \
    "v_pk_fma_f32 v[0:1], v[128:129], v[24:25], v[0:1] op_sel_hi:[1,0,1]\n\t" \
    "v_pk_fma_f32 v[2:3], v[130:131], v[24:25], v[2:3] op_sel_hi:[1,0,1]\n\t" \
    "v_pk_fma_f32 v[4:5], v[132:133], v[24:25], v[4:5] op_sel_hi:[1,0,1]\n\t" \
    "v_pk_fma_f32 v[6:7], v[134:135], v[24:25], v[6:7] op_sel_hi:[1,0,1]\n\t" \
    "v_pk_fma_f32 v[8:9], v[136:137], v[24:25], v[8:9] op_sel_hi:[1,0,1]\n\t" \
    "v_pk_fma_f32 v[10:11], v[138:139], v[24:25], v[10:11] op_sel_hi:[1,0,1]\n\t" \
    "v_pk_fma_f32 v[12:13], v[140:141], v[24:25], v[12:13] op_sel_hi:[1,0,1]\n\t" \
    "v_pk_fma_f32 v[14:15], v[142:143], v[24:25], v[14:15] op_sel_hi:[1,0,1]\n\t" \
    "v_pk_add_f32 v[16:17], v[0:1], v[2:3]\n\t" \
    "v_pk_add_f32 v[18:19], v[4:5], v[6:7]\n\t" \
    "v_pk_add_f32 v[20:21], v[8:9], v[10:11]\n\t" \
    "v_pk_add_f32 v[22:23], v[12:13], v[14:15]\n\t" \
    "v_pk_add_f32 v[16:17], v[16:17], v[18:19]\n\t" \
    "v_pk_add_f32 v[20:21], v[20:21], v[22:23]\n\t" \
    "v_mov_b32 v27, 0x207\n\t" \
    "v_pk_add_f32 v[16:17], v[16:17], v[20:21]\n\t" \
    "v_add_f32 v16, v16, v17\n\t" \
    "v_cmp_class_f32 vcc, v16, v27\n\t" \
    "s_nop 1\n\t" \
    "s_or_b64 %[bad], %[bad], vcc\n\t" \
    ""
#define P1_WWW \
    "s_cmp_eq_u32 %[c0], 6\n\t" \
    "s_cbranch_scc0 4f\n\t" \
    "buffer_store_dwordx4 v[200:203], v224, %[srd], %[so0] offen sc1\n\t" \
    "buffer_store_dwordx4 v[204:207], v224, %[srd], %[so0] offen offset:1024 sc1\n\t" \
    "buffer_store_dwordx4 v[208:211], v224, %[srd], %[so1] offen sc1\n\t" \
    "buffer_store_dwordx4 v[212:215], v224, %[srd], %[so1] offen offset:1024 sc1\n\t" \
    "buffer_store_dwordx4 v[216:219], v224, %[srd], %[so2] offen sc1\n\t" \
    "buffer_store_dwordx4 v[220:223], v224, %[srd], %[so2] offen offset:1024 sc1\n\t" \
    "4:\n\t" \
    "v_mov_b64 v[0:1], 0\n\t" \
    "v_mov_b64 v[2:3], 0\n\t" \
    "v_mov_b64 v[4:5], 0\n\t" \
    "v_mov_b64 v[6:7], 0\n\t" \
    "v_mov_b64 v[8:9], 0\n\t" \
    "v_mov_b64 v[10:11], 0\n\t" \
    "v_mov_b64 v[12:13], 0\n\t" \
    "v_mov_b64 v[14:15], 0\n\t" \
    "v_mov_b64 v[16:17], 0\n\t" \
    "v_mov_b64 v[18:19], 0\n\t" \
    "v_mov_b64 v[20:21], 0\n\t" \
    "v_mov_b64 v[22:23], 0\n\t" \
    "v_mov_b64 v[24:25], 0\n\t" \
    "v_mov_b64 v[26:27], 0\n\t" \
    "v_mov_b64 v[28:29], 0\n\t" \
    "v_mov_b64 v[30:31], 0\n\t" \
    "v_mov_b64 v[32:33], 0\n\t" \
    "v_mov_b64 v[34:35], 0\n\t" \
    "v_mov_b64 v[36:37], 0\n\t" \
    "v_mov_b64 v[38:39], 0\n\t" \
    "v_mov_b64 v[40:41], 0\n\t" \
    "v_mov_b64 v[42:43], 0\n\t" \
    "v_mov_b64 v[44:45], 0\n\t" \
    "v_mov_b64 v[46:47], 0\n\t" \
    "s_barrier\n\t" \
    "ds_read_b128 v[48:51], v80 offset:0\n\t" \
    "ds_read_b64_tr_b16 v[52:53], v84 offset:0\n\t" \
    "ds_read_b64_tr_b16 v[54:55], v84 offset:768\n\t" \
    "ds_read_b64_tr_b16 v[56:57], v84 offset:56\n\t" \
    "ds_read_b64_tr_b16 v[58:59], v84 offset:824\n\t" \
    "ds_read_b64_tr_b16 v[60:61], v84 offset:112\n\t" \
    "ds_read_b64_tr_b16 v[62:63], v84 offset:880\n\t" \
    "ds_read_b128 v[64:67], v81 offset:0\n\t" \
    "ds_read_b64_tr_b16 v[68:69], v84 offset:3072\n\t" \
    "ds_read_b64_tr_b16 v[70:71], v84 offset:3840\n\t" \
    "ds_read_b64_tr_b16 v[72:73], v84 offset:3128\n\t" \
    "ds_read_b64_tr_b16 v[74:75], v84 offset:3896\n\t" \
    "ds_read_b64_tr_b16 v[76:77], v84 offset:3184\n\t" \
    "ds_read_b64_tr_b16 v[78:79], v84 offset:3952\n\t" \
    "1:\n\t" \
    "s_waitcnt lgkmcnt(7)\n\t" \
    "v_mfma_f32_32x32x16_bf16 v[0:15], v[52:55], v[48:51], v[0:15]\n\t" \
    "ds_read_b128 v[88:91], v82 offset:0\n\t" \
    "ds_read_b64_tr_b16 v[92:93], v84 offset:6144\n\t" \
    "ds_read_b64_tr_b16 v[94:95], v84 offset:6912\n\t" \
    "v_mfma_f32_32x32x16_bf16 v[16:31], v[56:59], v[48:51], v[16:31]\n\t" \
    "ds_read_b64_tr_b16 v[96:97], v84 offset:6200\n\t" \
    "ds_read_b64_tr_b16 v[98:99], v84 offset:6968\n\t" \
    "v_mfma_f32_32x32x16_bf16 v[32:47], v[60:63], v[48:51], v[32:47]\n\t" \
    "ds_read_b64_tr_b16 v[100:101], v84 offset:6256\n\t" \
    "ds_read_b64_tr_b16 v[102:103], v84 offset:7024\n\t" \
    "s_waitcnt lgkmcnt(7)\n\t" \
    "v_mfma_f32_32x32x16_bf16 v[0:15], v[68:71], v[64:67], v[0:15]\n\t" \
    "ds_read_b128 v[104:107], v83 offset:0\n\t" \
    "ds_read_b64_tr_b16 v[108:109], v84 offset:9216\n\t" \
    "ds_read_b64_tr_b16 v[110:111], v84 offset:9984\n\t" \
    "v_mfma_f32_32x32x16_bf16 v[16:31], v[72:75], v[64:67], v[16:31]\n\t" \
    "ds_read_b64_tr_b16 v[112:113], v84 offset:9272\n\t" \
    "ds_read_b64_tr_b16 v[114:115], v84 offset:10040\n\t" \
    "v_mfma_f32_32x32x16_bf16 v[32:47], v[76:79], v[64:67], v[32:47]\n\t" \
    "ds_read_b64_tr_b16 v[116:117], v84 offset:9328\n\t" \
    "ds_read_b64_tr_b16 v[118:119], v84 offset:10096\n\t" \
    "s_add_u32 %[slot], %[slot], 1\n\t" \
    "s_cmp_eq_u32 %[slot], 5\n\t" \
    "s_mov_b32 %[dl], 28672\n\t" \
    "s_cselect_b32 %[dl], 0xfffe4000, %[dl]\n\t" \
    "s_cselect_b32 %[slot], 0, %[slot]\n\t" \
    "v_add_u32 v80, %[dl], v80\n\t" \
    "v_add_u32 v81, %[dl], v81\n\t" \
    "v_add_u32 v82, %[dl], v82\n\t" \
    "v_add_u32 v83, %[dl], v83\n\t" \
    "v_add_u32 v84, %[dl], v84\n\t" \
    "s_sub_u32 %[cnt], %[cnt], 1\n\t" \
    "s_cmp_eq_u32 %[cnt], 0\n\t" \
    "s_cbranch_scc1 3f\n\t" \
    "s_waitcnt lgkmcnt(0)\n\t" \
    "s_cmp_lg_u32 %[cnt], %[wv]\n\t" \
    "s_cbranch_scc1 2f\n\t" \
    "s_waitcnt vmcnt(0)\n\t" \
    "2:\n\t" \
    "s_barrier\n\t" \
    "v_mfma_f32_32x32x16_bf16 v[0:15], v[92:95], v[88:91], v[0:15]\n\t" \
    "ds_read_b128 v[48:51], v80 offset:0\n\t" \
    "ds_read_b64_tr_b16 v[52:53], v84 offset:0\n\t" \
    "ds_read_b64_tr_b16 v[54:55], v84 offset:768\n\t" \
    "v_mfma_f32_32x32x16_bf16 v[16:31], v[96:99], v[88:91], v[16:31]\n\t" \
    "ds_read_b64_tr_b16 v[56:57], v84 offset:56\n\t" \
    "ds_read_b64_tr_b16 v[58:59], v84 offset:824\n\t" \
    "v_mfma_f32_32x32x16_bf16 v[32:47], v[100:103], v[88:91], v[32:47]\n\t" \
    "ds_read_b64_tr_b16 v[60:61], v84 offset:112\n\t" \
    "ds_read_b64_tr_b16 v[62:63], v84 offset:880\n\t" \
    "v_mfma_f32_32x32x16_bf16 v[0:15], v[108:111], v[104:107], v[0:15]\n\t" \
    "ds_read_b128 v[64:67], v81 offset:0\n\t" \
    "ds_read_b64_tr_b16 v[68:69], v84 offset:3072\n\t" \
    "ds_read_b64_tr_b16 v[70:71], v84 offset:3840\n\t" \
    "v_mfma_f32_32x32x16_bf16 v[16:31], v[112:115], v[104:107], v[16:31]\n\t" \
    "ds_read_b64_tr_b16 v[72:73], v84 offset:3128\n\t" \
    "ds_read_b64_tr_b16 v[74:75], v84 offset:3896\n\t" \
    "v_mfma_f32_32x32x16_bf16 v[32:47], v[116:119], v[104:107], v[32:47]\n\t" \
    "ds_read_b64_tr_b16 v[76:77], v84 offset:3184\n\t" \
    "ds_read_b64_tr_b16 v[78:79], v84 offset:3952\n\t" \
    "s_branch 1b\n\t" \
    "3:\n\t" \
    "s_waitcnt lgkmcnt(7)\n\t" \
    "v_mfma_f32_32x32x16_bf16 v[0:15], v[92:95], v[88:91], v[0:15]\n\t" \
    "v_mfma_f32_32x32x16_bf16 v[16:31], v[96:99], v[88:91], v[16:31]\n\t" \
    "v_mfma_f32_32x32x16_bf16 v[32:47], v[100:103], v[88:91], v[32:47]\n\t" \
    "s_waitcnt lgkmcnt(0)\n\t" \
    "v_mfma_f32_32x32x16_bf16 v[0:15], v[108:111], v[104:107], v[0:15]\n\t" \
    "v_mfma_f32_32x32x16_bf16 v[16:31], v[112:115], v[104:107], v[16:31]\n\t" \
    "v_mfma_f32_32x32x16_bf16 v[32:47], v[116:119], v[104:107], v[32:47]\n\t" \
    "s_nop 7\n\t" \
    "s_nop 1\n\t" \
    "s_bitcmp1_b32 %[qm], 0\n\t" \
    "s_cselect_b32 %[t0], 0x3e8b97f5, 1.0\n\t" \
    "s_cselect_b32 %[t1], 0, 0x3f80\n\t" \
    "v_mov_b32 v48, %[t0]\n\t" \
    "v_mov_b32 v50, %[t1]\n\t" \
    "v_pk_mul_f32 v[0:1], v[0:1], v[48:49] op_sel_hi:[1,0]\n\t" \
    "v_pk_mul_f32 v[2:3], v[2:3], v[48:49] op_sel_hi:[1,0]\n\t" \
    "v_pk_mul_f32 v[4:5], v[4:5], v[48:49] op_sel_hi:[1,0]\n\t" \
    "v_pk_mul_f32 v[6:7], v[6:7], v[48:49] op_sel_hi:[1,0]\n\t" \
    "v_pk_mul_f32 v[8:9], v[8:9], v[48:49] op_sel_hi:[1,0]\n\t" \
    "v_pk_mul_f32 v[10:11], v[10:11], v[48:49] op_sel_hi:[1,0]\n\t" \
    "v_pk_mul_f32 v[12:13], v[12:13], v[48:49] op_sel_hi:[1,0]\n\t" \
    "v_pk_mul_f32 v[14:15], v[14:15], v[48:49] op_sel_hi:[1,0]\n\t" \
    "v_cvt_pk_bf16_f32 v200, v0, v1\n\t" \
    "v_cvt_pk_bf16_f32 v201, v2, v3\n\t" \
    "v_cvt_pk_bf16_f32 v202, v4, v5\n\t" \
    "v_cvt_pk_bf16_f32 v203, v6, v7\n\t" \
    "v_cvt_pk_bf16_f32 v204, v8, v9\n\t" \
    "v_cvt_pk_bf16_f32 v205, v10, v11\n\t" \
    "v_cvt_pk_bf16_f32 v206, v12, v13\n\t" \
    "v_cvt_pk_bf16_f32 v207, v14, v15\n\t" \
    "v_cndmask_b32_e64 v206, v206, v50, %[hi]\n\t" \
    "v_cndmask_b32_e64 v207, v207, 0, %[hi]\n\t" \
    "s_bitcmp1_b32 %[qm], 1\n\t" \
    "s_cselect_b32 %[t0], 0x3e8b97f5, 1.0\n\t" \
    "s_cselect_b32 %[t1], 0, 0x3f80\n\t" \
    "v_mov_b32 v48, %[t0]\n\t" \
    "v_mov_b32 v50, %[t1]\n\t" \
    "v_pk_mul_f32 v[16:17], v[16:17], v[48:49] op_sel_hi:[1,0]\n\t" \
    "v_pk_mul_f32 v[18:19], v[18:19], v[48:49] op_sel_hi:[1,0]\n\t" \
    "v_pk_mul_f32 v[20:21], v[20:21], v[48:49] op_sel_hi:[1,0]\n\t" \
    "v_pk_mul_f32 v[22:23], v[22:23], v[48:49] op_sel_hi:[1,0]\n\t" \
    "v_pk_mul_f32 v[24:25], v[24:25], v[48:49] op_sel_hi:[1,0]\n\t" \
    "v_pk_mul_f32 v[26:27], v[26:27], v[48:49] op_sel_hi:[1,0]\n\t" \
    "v_pk_mul_f32 v[28:29], v[28:29], v[48:49] op_sel_hi:[1,0]\n\t" \
    "v_pk_mul_f32 v[30:31], v[30:31], v[48:49] op_sel_hi:[1,0]\n\t" \
    "v_cvt_pk_bf16_f32 v208, v16, v17\n\t" \
    "v_cvt_pk_bf16_f32 v209, v18, v19\n\t" \
    "v_cvt_pk_bf16_f32 v210, v20, v21\n\t" \
    "v_cvt_pk_bf16_f32 v211, v22, v23\n\t" \
    "v_cvt_pk_bf16_f32 v212, v24, v25\n\t" \
    "v_cvt_pk_bf16_f32 v213, v26, v27\n\t" \
    "v_cvt_pk_bf16_f32 v214, v28, v29\n\t" \
    "v_cvt_pk_bf16_f32 v215, v30, v31\n\t" \
    "v_cndmask_b32_e64 v214, v214, v50, %[hi]\n\t" \
    "v_cndmask_b32_e64 v215, v215, 0, %[hi]\n\t" \
    "s_nop 3\n\t" \
    "s_bitcmp1_b32 %[qm], 2\n\t" \
    "s_cselect_b32 %[t0], 0x3e8b97f5, 1.0\n\t" \
    "s_cselect_b32 %[t1], 0, 0x3f80\n\t" \
    "v_mov_b32 v48, %[t0]\n\t" \
    "v_mov_b32 v50, %[t1]\n\t" \
    "v_pk_mul_f32 v[32:33], v[32:33], v[48:49] op_sel_hi:[1,0]\n\t" \
    "v_pk_mul_f32 v[34:35], v[34:35], v[48:49] op_sel_hi:[1,0]\n\t" \
    "v_pk_mul_f32 v[36:37], v[36:37], v[48:49] op_sel_hi:[1,0]\n\t" \
    "v_pk_mul_f32 v[38:39], v[38:39], v[48:49] op_sel_hi:[1,0]\n\t" \
    "v_pk_mul_f32 v[40:41], v[40:41], v[48:49] op_sel_hi:[1,0]\n\t" \
    "v_pk_mul_f32 v[42:43], v[42:43], v[48:49] op_sel_hi:[1,0]\n\t" \
    "v_pk_mul_f32 v[44:45], v[44:45], v[48:49] op_sel_hi:[1,0]\n\t" \
    "v_pk_mul_f32 v[46:47], v[46:47], v[48:49] op_sel_hi:[1,0]\n\t" \
    "v_cvt_pk_bf16_f32 v216, v32, v33\n\t" \
    "v_cvt_pk_bf16_f32 v217, v34, v35\n\t" \
    "v_cvt_pk_bf16_f32 v218, v36, v37\n\t" \
    "v_cvt_pk_bf16_f32 v219, v38, v39\n\t" \
    "v_cvt_pk_bf16_f32 v220, v40, v41\n\t" \
    "v_cvt_pk_bf16_f32 v221, v42, v43\n\t" \
    "v_cvt_pk_bf16_f32 v222, v44, v45\n\t" \
    "v_cvt_pk_bf16_f32 v223, v46, v47\n\t" \
    "v_cndmask_b32_e64 v222, v222, v50, %[hi]\n\t" \
    "v_cndmask_b32_e64 v223, v223, 0, %[hi]\n\t" \
    ""
#define P1_WWX \
    "s_cmp_eq_u32 %[c0], 6\n\t" \
    "s_cbranch_scc0 4f\n\t" \
    "buffer_store_dwordx4 v[200:203], v224, %[srd], %[so0] offen sc1\n\t" \
    "buffer_store_dwordx4 v[204:207], v224, %[srd], %[so0] offen offset:1024 sc1\n\t" \
    "buffer_store_dwordx4 v[208:211], v224, %[srd], %[so1] offen sc1\n\t" \
    "buffer_store_dwordx4 v[212:215], v224, %[srd], %[so1] offen offset:1024 sc1\n\t" \
    "buffer_store_dwordx4 v[216:219], v224, %[srd], %[so2] offen sc1\n\t" \
    "buffer_store_dwordx4 v[220:223], v224, %[srd], %[so2] offen offset:1024 sc1\n\t" \
    "4:\n\t" \
    "v_mov_b64 v[0:1], 0\n\t" \
    "v_mov_b64 v[2:3], 0\n\t" \
    "v_mov_b64 v[4:5], 0\n\t" \
    "v_mov_b64 v[6:7], 0\n\t" \
    "v_mov_b64 v[8:9], 0\n\t" \
    "v_mov_b64 v[10:11], 0\n\t" \
    "v_mov_b64 v[12:13], 0\n\t" \
    "v_mov_b64 v[14:15], 0\n\t" \
    "v_mov_b64 v[16:17], 0\n\t" \
    "v_mov_b64 v[18:19], 0\n\t" \
    "v_mov_b64 v[20:21], 0\n\t" \
    "v_mov_b64 v[22:23], 0\n\t" \
    "v_mov_b64 v[24:25], 0\n\t" \
    "v_mov_b64 v[26:27], 0\n\t" \
    "v_mov_b64 v[28:29], 0\n\t" \
    "v_mov_b64 v[30:31], 0\n\t" \
    "v_mov_b64 v[32:33], 0\n\t" \
    "v_mov_b64 v[34:35], 0\n\t" \
    "v_mov_b64 v[36:37], 0\n\t" \
    "v_mov_b64 v[38:39], 0\n\t" \
    "v_mov_b64 v[40:41], 0\n\t" \
    "v_mov_b64 v[42:43], 0\n\t" \
    "v_mov_b64 v[44:45], 0\n\t" \
    "v_mov_b64 v[46:47], 0\n\t" \
    "s_barrier\n\t" \
    "ds_read_b128 v[48:51], v80 offset:0\n\t" \
    "ds_read_b64_tr_b16 v[52:53], v84 offset:0\n\t" \
    "ds_read_b64_tr_b16 v[54:55], v84 offset:768\n\t" \
    "ds_read_b64_tr_b16 v[56:57], v84 offset:56\n\t" \
    "ds_read_b64_tr_b16 v[58:59], v84 offset:824\n\t" \
    "ds_read_b64_tr_b16 v[60:61], v84 offset:112\n\t" \
    "ds_read_b64_tr_b16 v[62:63], v84 offset:880\n\t" \
    "ds_read_b128 v[64:67], v81 offset:0\n\t" \
    "ds_read_b64_tr_b16 v[68:69], v84 offset:3072\n\t" \
    "ds_read_b64_tr_b16 v[70:71], v84 offset:3840\n\t" \
    "ds_read_b64_tr_b16 v[72:73], v84 offset:3128\n\t" \
    "ds_read_b64_tr_b16 v[74:75], v84 offset:3896\n\t" \
    "ds_read_b64_tr_b16 v[76:77], v84 offset:3184\n\t" \
    "ds_read_b64_tr_b16 v[78:79], v84 offset:3952\n\t" \
    "1:\n\t" \
    "s_waitcnt lgkmcnt(7)\n\t" \
    "v_mfma_f32_32x32x16_bf16 v[0:15], v[52:55], v[48:51], v[0:15]\n\t" \
    "ds_read_b128 v[88:91], v82 offset:0\n\t" \
    "ds_read_b64_tr_b16 v[92:93], v84 offset:6144\n\t" \
    "ds_read_b64_tr_b16 v[94:95], v84 offset:6912\n\t" \
    "v_mfma_f32_32x32x16_bf16 v[16:31], v[56:59], v[48:51], v[16:31]\n\t" \
    "ds_read_b64_tr_b16 v[96:97], v84 offset:6200\n\t" \
    "ds_read_b64_tr_b16 v[98:99], v84 offset:6968\n\t" \
    "v_mfma_f32_32x32x16_bf16 v[32:47], v[48:51], v[60:63], v[32:47]\n\t" \
    "ds_read_b64_tr_b16 v[100:101], v84 offset:6256\n\t" \
    "ds_read_b64_tr_b16 v[102:103], v84 offset:7024\n\t" \
    "s_waitcnt lgkmcnt(7)\n\t" \
    "v_mfma_f32_32x32x16_bf16 v[0:15], v[68:71], v[64:67], v[0:15]\n\t" \
    "ds_read_b128 v[104:107], v83 offset:0\n\t" \
    "ds_read_b64_tr_b16 v[108:109], v84 offset:9216\n\t" \
    "ds_read_b64_tr_b16 v[110:111], v84 offset:9984\n\t" \
    "v_mfma_f32_32x32x16_bf16 v[16:31], v[72:75], v[64:67], v[16:31]\n\t" \
    "ds_read_b64_tr_b16 v[112:113], v84 offset:9272\n\t" \
    "ds_read_b64_tr_b16 v[114:115], v84 offset:10040\n\t" \
    "v_mfma_f32_32x32x16_bf16 v[32:47], v[64:67], v[76:79], v[32:47]\n\t" \
    "ds_read_b64_tr_b16 v[116:117], v84 offset:9328\n\t" \
    "ds_read_b64_tr_b16 v[118:119], v84 offset:10096\n\t" \
    "s_add_u32 %[slot], %[slot], 1\n\t" \
    "s_cmp_eq_u32 %[slot], 5\n\t" \
    "s_mov_b32 %[dl], 28672\n\t" \
    "s_cselect_b32 %[dl], 0xfffe4000, %[dl]\n\t" \
    "s_cselect_b32 %[slot], 0, %[slot]\n\t" \
    "v_add_u32 v80, %[dl], v80\n\t" \
    "v_add_u32 v81, %[dl], v81\n\t" \
    "v_add_u32 v82, %[dl], v82\n\t" \
    "v_add_u32 v83, %[dl], v83\n\t" \
    "v_add_u32 v84, %[dl], v84\n\t" \
    "s_sub_u32 %[cnt], %[cnt], 1\n\t" \
    "s_cmp_eq_u32 %[cnt], 0\n\t" \
    "s_cbranch_scc1 3f\n\t" \
    "s_waitcnt lgkmcnt(0)\n\t" \
    "s_cmp_lg_u32 %[cnt], %[wv]\n\t" \
    "s_cbranch_scc1 2f\n\t" \
    "s_waitcnt vmcnt(0)\n\t" \
    "2:\n\t" \
    "s_barrier\n\t" \
    "v_mfma_f32_32x32x16_bf16 v[0:15], v[92:95], v[88:91], v[0:15]\n\t" \
    "ds_read_b128 v[48:51], v80 offset:0\n\t" \
    "ds_read_b64_tr_b16 v[52:53], v84 offset:0\n\t" \
    "ds_read_b64_tr_b16 v[54:55], v84 offset:768\n\t" \
    "v_mfma_f32_32x32x16_bf16 v[16:31], v[96:99], v[88:91], v[16:31]\n\t" \
    "ds_read_b64_tr_b16 v[56:57], v84 offset:56\n\t" \
    "ds_read_b64_tr_b16 v[58:59], v84 offset:824\n\t" \
    "v_mfma_f32_32x32x16_bf16 v[32:47], v[88:91], v[100:103], v[32:47]\n\t" \
    "ds_read_b64_tr_b16 v[60:61], v84 offset:112\n\t" \
    "ds_read_b64_tr_b16 v[62:63], v84 offset:880\n\t" \
    "v_mfma_f32_32x32x16_bf16 v[0:15], v[108:111], v[104:107], v[0:15]\n\t" \
    "ds_read_b128 v[64:67], v81 offset:0\n\t" \
    "ds_read_b64_tr_b16 v[68:69], v84 offset:3072\n\t" \
    "ds_read_b64_tr_b16 v[70:71], v84 offset:3840\n\t" \
    "v_mfma_f32_32x32x16_bf16 v[16:31], v[112:115], v[104:107], v[16:31]\n\t" \
    "ds_read_b64_tr_b16 v[72:73], v84 offset:3128\n\t" \
    "ds_read_b64_tr_b16 v[74:75], v84 offset:3896\n\t" \
    "v_mfma_f32_32x32x16_bf16 v[32:47], v[104:107], v[116:119], v[32:47]\n\t" \
    "ds_read_b64_tr_b16 v[76:77], v84 offset:3184\n\t" \
    "ds_read_b64_tr_b16 v[78:79], v84 offset:3952\n\t" \
    "s_branch 1b\n\t" \
    "3:\n\t" \
    "s_waitcnt lgkmcnt(7)\n\t" \
    "v_mfma_f32_32x32x16_bf16 v[0:15], v[92:95], v[88:91], v[0:15]\n\t" \
    "v_mfma_f32_32x32x16_bf16 v[16:31], v[96:99], v[88:91], v[16:31]\n\t" \
    "v_mfma_f32_32x32x16_bf16 v[32:47], v[88:91], v[100:103], v[32:47]\n\t" \
    "s_waitcnt lgkmcnt(0)\n\t" \
    "v_mfma_f32_32x32x16_bf16 v[0:15], v[108:111], v[104:107], v[0:15]\n\t" \
    "v_mfma_f32_32x32x16_bf16 v[16:31], v[112:115], v[104:107], v[16:31]\n\t" \
    "v_mfma_f32_32x32x16_bf16 v[32:47], v[104:107], v[116:119], v[32:47]\n\t" \
    "s_nop 7\n\t" \
    "s_nop 1\n\t" \
    "s_bitcmp1_b32 %[qm], 0\n\t" \
    "s_cselect_b32 %[t0], 0x3e8b97f5, 1.0\n\t" \
    "s_cselect_b32 %[t1], 0, 0x3f80\n\t" \
    "v_mov_b32 v48, %[t0]\n\t" \
    "v_mov_b32 v50, %[t1]\n\t" \
    "v_pk_mul_f32 v[0:1], v[0:1], v[48:49] op_sel_hi:[1,0]\n\t" \
    "v_pk_mul_f32 v[2:3], v[2:3], v[48:49] op_sel_hi:[1,0]\n\t" \
    "v_pk_mul_f32 v[4:5], v[4:5], v[48:49] op_sel_hi:[1,0]\n\t" \
    "v_pk_mul_f32 v[6:7], v[6:7], v[48:49] op_sel_hi:[1,0]\n\t" \
    "v_pk_mul_f32 v[8:9], v[8:9], v[48:49] op_sel_hi:[1,0]\n\t" \
    "v_pk_mul_f32 v[10:11], v[10:11], v[48:49] op_sel_hi:[1,0]\n\t" \
    "v_pk_mul_f32 v[12:13], v[12:13], v[48:49] op_sel_hi:[1,0]\n\t" \
    "v_pk_mul_f32 v[14:15], v[14:15], v[48:49] op_sel_hi:[1,0]\n\t" \
    "v_cvt_pk_bf16_f32 v200, v0, v1\n\t" \
    "v_cvt_pk_bf16_f32 v201, v2, v3\n\t" \
    "v_cvt_pk_bf16_f32 v202, v4, v5\n\t" \
    "v_cvt_pk_bf16_f32 v203, v6, v7\n\t" \
    "v_cvt_pk_bf16_f32 v204, v8, v9\n\t" \
    "v_cvt_pk_bf16_f32 v205, v10, v11\n\t" \
    "v_cvt_pk_bf16_f32 v206, v12, v13\n\t" \
    "v_cvt_pk_bf16_f32 v207, v14, v15\n\t" \
    "v_cndmask_b32_e64 v206, v206, v50, %[hi]\n\t" \
    "v_cndmask_b32_e64 v207, v207, 0, %[hi]\n\t" \
    "s_bitcmp1_b32 %[qm], 1\n\t" \
    "s_cselect_b32 %[t0], 0x3e8b97f5, 1.0\n\t" \
    "s_cselect_b32 %[t1], 0, 0x3f80\n\t" \
    "v_mov_b32 v48, %[t0]\n\t" \
    "v_mov_b32 v50, %[t1]\n\t" \
    "v_pk_mul_f32 v[16:17], v[16:17], v[48:49] op_sel_hi:[1,0]\n\t" \
    "v_pk_mul_f32 v[18:19], v[18:19], v[48:49] op_sel_hi:[1,0]\n\t" \
    "v_pk_mul_f32 v[20:21], v[20:21], v[48:49] op_sel_hi:[1,0]\n\t" \
    "v_pk_mul_f32 v[22:23], v[22:23], v[48:49] op_sel_hi:[1,0]\n\t" \
    "v_pk_mul_f32 v[24:25], v[24:25], v[48:49] op_sel_hi:[1,0]\n\t" \
    "v_pk_mul_f32 v[26:27], v[26:27], v[48:49] op_sel_hi:[1,0]\n\t" \
    "v_pk_mul_f32 v[28:29], v[28:29], v[48:49] op_sel_hi:[1,0]\n\t" \
    "v_pk_mul_f32 v[30:31], v[30:31], v[48:49] op_sel_hi:[1,0]\n\t" \
    "v_cvt_pk_bf16_f32 v208, v16, v17\n\t" \
    "v_cvt_pk_bf16_f32 v209, v18, v19\n\t" \
    "v_cvt_pk_bf16_f32 v210, v20, v21\n\t" \
    "v_cvt_pk_bf16_f32 v211, v22, v23\n\t" \
    "v_cvt_pk_bf16_f32 v212, v24, v25\n\t" \
    "v_cvt_pk_bf16_f32 v213, v26, v27\n\t" \
    "v_cvt_pk_bf16_f32 v214, v28, v29\n\t" \
    "v_cvt_pk_bf16_f32 v215, v30, v31\n\t" \
    "v_cndmask_b32_e64 v214, v214, v50, %[hi]\n\t" \
    "v_cndmask_b32_e64 v215, v215, 0, %[hi]\n\t" \
    "s_nop 3\n\t" \
    "v_cvt_pk_bf16_f32 v216, v32, v33\n\t" \
    "v_cvt_pk_bf16_f32 v217, v34, v35\n\t" \
    "v_cvt_pk_bf16_f32 v218, v36, v37\n\t" \
    "v_cvt_pk_bf16_f32 v219, v38, v39\n\t" \
    "v_cvt_pk_bf16_f32 v220, v40, v41\n\t" \
    "v_cvt_pk_bf16_f32 v221, v42, v43\n\t" \
    "v_cvt_pk_bf16_f32 v222, v44, v45\n\t" \
    "v_cvt_pk_bf16_f32 v223, v46, v47\n\t" \
    "v_cndmask_b32_e64 v216, v216, %[vk], %[r28]\n\t" \
    "v_cndmask_b32_e64 v217, v217, %[vk], %[r28]\n\t" \
    "v_cndmask_b32_e64 v218, v218, %[vk], %[r28]\n\t" \
    "v_cndmask_b32_e64 v219, v219, %[vk], %[r28]\n\t" \
    "v_cndmask_b32_e64 v220, v220, %[vk], %[r28]\n\t" \
    "v_cndmask_b32_e64 v221, v221, %[vk], %[r28]\n\t" \
    "v_cndmask_b32_e64 v222, v222, %[vk], %[r28]\n\t" \
    "v_cndmask_b32_e64 v223, v223, %[vk], %[r28]\n\t" \
    ""
#define P1_XXX \
    "s_cmp_eq_u32 %[c0], 6\n\t" \
    "s_cbranch_scc0 4f\n\t" \
    "buffer_store_dwordx4 v[200:203], v224, %[srd], %[so0] offen sc1\n\t" \
    "buffer_store_dwordx4 v[204:207], v224, %[srd], %[so0] offen offset:1024 sc1\n\t" \
    "buffer_store_dwordx4 v[208:211], v224, %[srd], %[so1] offen sc1\n\t" \
    "buffer_store_dwordx4 v[212:215], v224, %[srd], %[so1] offen offset:1024 sc1\n\t" \
    "buffer_store_dwordx4 v[216:219], v224, %[srd], %[so2] offen sc1\n\t" \
    "buffer_store_dwordx4 v[220:223], v224, %[srd], %[so2] offen offset:1024 sc1\n\t" \
    "4:\n\t" \
    "v_mov_b64 v[0:1], 0\n\t" \
    "v_mov_b64 v[2:3], 0\n\t" \
    "v_mov_b64 v[4:5], 0\n\t" \
    "v_mov_b64 v[6:7], 0\n\t" \
    "v_mov_b64 v[8:9], 0\n\t" \
    "v_mov_b64 v[10:11], 0\n\t" \
    "v_mov_b64 v[12:13], 0\n\t" \
    "v_mov_b64 v[14:15], 0\n\t" \
    "v_mov_b64 v[16:17], 0\n\t" \
    "v_mov_b64 v[18:19], 0\n\t" \
    "v_mov_b64 v[20:21], 0\n\t" \
    "v_mov_b64 v[22:23], 0\n\t" \
    "v_mov_b64 v[24:25], 0\n\t" \
    "v_mov_b64 v[26:27], 0\n\t" \
    "v_mov_b64 v[28:29], 0\n\t" \
    "v_mov_b64 v[30:31], 0\n\t" \
    "v_mov_b64 v[32:33], 0\n\t" \
    "v_mov_b64 v[34:35], 0\n\t" \
    "v_mov_b64 v[36:37], 0\n\t" \
    "v_mov_b64 v[38:39], 0\n\t" \
    "v_mov_b64 v[40:41], 0\n\t" \
    "v_mov_b64 v[42:43], 0\n\t" \
    "v_mov_b64 v[44:45], 0\n\t" \
    "v_mov_b64 v[46:47], 0\n\t" \
    "s_barrier\n\t" \
    "ds_read_b128 v[48:51], v80 offset:0\n\t" \
    "ds_read_b64_tr_b16 v[52:53], v84 offset:0\n\t" \
    "ds_read_b64_tr_b16 v[54:55], v84 offset:768\n\t" \
    "ds_read_b64_tr_b16 v[56:57], v84 offset:56\n\t" \
    "ds_read_b64_tr_b16 v[58:59], v84 offset:824\n\t" \
    "ds_read_b64_tr_b16 v[60:61], v84 offset:112\n\t" \
    "ds_read_b64_tr_b16 v[62:63], v84 offset:880\n\t" \
    "ds_read_b128 v[64:67], v81 offset:0\n\t" \
    "ds_read_b64_tr_b16 v[68:69], v84 offset:3072\n\t" \
    "ds_read_b64_tr_b16 v[70:71], v84 offset:3840\n\t" \
    "ds_read_b64_tr_b16 v[72:73], v84 offset:3128\n\t" \
    "ds_read_b64_tr_b16 v[74:75], v84 offset:3896\n\t" \
    "ds_read_b64_tr_b16 v[76:77], v84 offset:3184\n\t" \
    "ds_read_b64_tr_b16 v[78:79], v84 offset:3952\n\t" \
    "1:\n\t" \
    "s_waitcnt lgkmcnt(7)\n\t" \
    "v_mfma_f32_32x32x16_bf16 v[0:15], v[48:51], v[52:55], v[0:15]\n\t" \
    "ds_read_b128 v[88:91], v82 offset:0\n\t" \
    "ds_read_b64_tr_b16 v[92:93], v84 offset:6144\n\t" \
    "ds_read_b64_tr_b16 v[94:95], v84 offset:6912\n\t" \
    "v_mfma_f32_32x32x16_bf16 v[16:31], v[48:51], v[56:59], v[16:31]\n\t" \
    "ds_read_b64_tr_b16 v[96:97], v84 offset:6200\n\t" \
    "ds_read_b64_tr_b16 v[98:99], v84 offset:6968\n\t" \
    "v_mfma_f32_32x32x16_bf16 v[32:47], v[48:51], v[60:63], v[32:47]\n\t" \
    "ds_read_b64_tr_b16 v[100:101], v84 offset:6256\n\t" \
    "ds_read_b64_tr_b16 v[102:103], v84 offset:7024\n\t" \
    "s_waitcnt lgkmcnt(7)\n\t" \
    "v_mfma_f32_32x32x16_bf16 v[0:15], v[64:67], v[68:71], v[0:15]\n\t" \
    "ds_read_b128 v[104:107], v83 offset:0\n\t" \
    "ds_read_b64_tr_b16 v[108:109], v84 offset:9216\n\t" \
    "ds_read_b64_tr_b16 v[110:111], v84 offset:9984\n\t" \
    "v_mfma_f32_32x32x16_bf16 v[16:31], v[64:67], v[72:75], v[16:31]\n\t" \
    "ds_read_b64_tr_b16 v[112:113], v84 offset:9272\n\t" \
    "ds_read_b64_tr_b16 v[114:115], v84 offset:10040\n\t" \
    "v_mfma_f32_32x32x16_bf16 v[32:47], v[64:67], v[76:79], v[32:47]\n\t" \
    "ds_read_b64_tr_b16 v[116:117], v84 offset:9328\n\t" \
    "ds_read_b64_tr_b16 v[118:119], v84 offset:10096\n\t" \
    "s_add_u32 %[slot], %[slot], 1\n\t" \
    "s_cmp_eq_u32 %[slot], 5\n\t" \
    "s_mov_b32 %[dl], 28672\n\t" \
    "s_cselect_b32 %[dl], 0xfffe4000, %[dl]\n\t" \
    "s_cselect_b32 %[slot], 0, %[slot]\n\t" \
    "v_add_u32 v80, %[dl], v80\n\t" \
    "v_add_u32 v81, %[dl], v81\n\t" \
    "v_add_u32 v82, %[dl], v82\n\t" \
    "v_add_u32 v83, %[dl], v83\n\t" \
    "v_add_u32 v84, %[dl], v84\n\t" \
    "s_sub_u32 %[cnt], %[cnt], 1\n\t" \
    "s_cmp_eq_u32 %[cnt], 0\n\t" \
    "s_cbranch_scc1 3f\n\t" \
    "s_waitcnt lgkmcnt(0)\n\t" \
    "s_cmp_lg_u32 %[cnt], %[wv]\n\t" \
    "s_cbranch_scc1 2f\n\t" \
    "s_waitcnt vmcnt(0)\n\t" \
    "2:\n\t" \
    "s_barrier\n\t" \
    "v_mfma_f32_32x32x16_bf16 v[0:15], v[88:91], v[92:95], v[0:15]\n\t" \
    "ds_read_b128 v[48:51], v80 offset:0\n\t" \
    "ds_read_b64_tr_b16 v[52:53], v84 offset:0\n\t" \
    "ds_read_b64_tr_b16 v[54:55], v84 offset:768\n\t" \
    "v_mfma_f32_32x32x16_bf16 v[16:31], v[88:91], v[96:99], v[16:31]\n\t" \
    "ds_read_b64_tr_b16 v[56:57], v84 offset:56\n\t" \
    "ds_read_b64_tr_b16 v[58:59], v84 offset:824\n\t" \
    "v_mfma_f32_32x32x16_bf16 v[32:47], v[88:91], v[100:103], v[32:47]\n\t" \
    "ds_read_b64_tr_b16 v[60:61], v84 offset:112\n\t" \
    "ds_read_b64_tr_b16 v[62:63], v84 offset:880\n\t" \
    "v_mfma_f32_32x32x16_bf16 v[0:15], v[104:107], v[108:111], v[0:15]\n\t" \
    "ds_read_b128 v[64:67], v81 offset:0\n\t" \
    "ds_read_b64_tr_b16 v[68:69], v84 offset:3072\n\t" \
    "ds_read_b64_tr_b16 v[70:71], v84 offset:3840\n\t" \
    "v_mfma_f32_32x32x16_bf16 v[16:31], v[104:107], v[112:115], v[16:31]\n\t" \
    "ds_read_b64_tr_b16 v[72:73], v84 offset:3128\n\t" \
    "ds_read_b64_tr_b16 v[74:75], v84 offset:3896\n\t" \
    "v_mfma_f32_32x32x16_bf16 v[32:47], v[104:107], v[116:119], v[32:47]\n\t" \
    "ds_read_b64_tr_b16 v[76:77], v84 offset:3184\n\t" \
    "ds_read_b64_tr_b16 v[78:79], v84 offset:3952\n\t" \
    "s_branch 1b\n\t" \
    "3:\n\t" \
    "s_waitcnt lgkmcnt(7)\n\t" \
    "v_mfma_f32_32x32x16_bf16 v[0:15], v[88:91], v[92:95], v[0:15]\n\t" \
    "v_mfma_f32_32x32x16_bf16 v[16:31], v[88:91], v[96:99], v[16:31]\n\t" \
    "v_mfma_f32_32x32x16_bf16 v[32:47], v[88:91], v[100:103], v[32:47]\n\t" \
    "s_waitcnt lgkmcnt(0)\n\t" \
    "v_mfma_f32_32x32x16_bf16 v[0:15], v[104:107], v[108:111], v[0:15]\n\t" \
    "v_mfma_f32_32x32x16_bf16 v[16:31], v[104:107], v[112:115], v[16:31]\n\t" \
    "v_mfma_f32_32x32x16_bf16 v[32:47], v[104:107], v[116:119], v[32:47]\n\t" \
    "s_nop 7\n\t" \
    "s_nop 1\n\t" \
    "v_cvt_pk_bf16_f32 v200, v0, v1\n\t" \
    "v_cvt_pk_bf16_f32 v201, v2, v3\n\t" \
    "v_cvt_pk_bf16_f32 v202, v4, v5\n\t" \
    "v_cvt_pk_bf16_f32 v203, v6, v7\n\t" \
    "v_cvt_pk_bf16_f32 v204, v8, v9\n\t" \
    "v_cvt_pk_bf16_f32 v205, v10, v11\n\t" \
    "v_cvt_pk_bf16_f32 v206, v12, v13\n\t" \
    "v_cvt_pk_bf16_f32 v207, v14, v15\n\t" \
    "v_cndmask_b32_e64 v200, v200, %[vk], %[r28]\n\t" \
    "v_cndmask_b32_e64 v201, v201, %[vk], %[r28]\n\t" \
    "v_cndmask_b32_e64 v202, v202, %[vk], %[r28]\n\t" \
    "v_cndmask_b32_e64 v203, v203, %[vk], %[r28]\n\t" \
    "v_cndmask_b32_e64 v204, v204, %[vk], %[r28]\n\t" \
    "v_cndmask_b32_e64 v205, v205, %[vk], %[r28]\n\t" \
    "v_cndmask_b32_e64 v206, v206, %[vk], %[r28]\n\t" \
    "v_cndmask_b32_e64 v207, v207, %[vk], %[r28]\n\t" \
    "v_cvt_pk_bf16_f32 v208, v16, v17\n\t" \
    "v_cvt_pk_bf16_f32 v209, v18, v19\n\t" \
    "v_cvt_pk_bf16_f32 v210, v20, v21\n\t" \
    "v_cvt_pk_bf16_f32 v211, v22, v23\n\t" \
    "v_cvt_pk_bf16_f32 v212, v24, v25\n\t" \
    "v_cvt_pk_bf16_f32 v213, v26, v27\n\t" \
    "v_cvt_pk_bf16_f32 v214, v28, v29\n\t" \
    "v_cvt_pk_bf16_f32 v215, v30, v31\n\t" \
    "v_cndmask_b32_e64 v208, v208, %[vk], %[r28]\n\t" \
    "v_cndmask_b32_e64 v209, v209, %[vk], %[r28]\n\t" \
    "v_cndmask_b32_e64 v210, v210, %[vk], %[r28]\n\t" \
    "v_cndmask_b32_e64 v211, v211, %[vk], %[r28]\n\t" \
    "v_cndmask_b32_e64 v212, v212, %[vk], %[r28]\n\t" \
    "v_cndmask_b32_e64 v213, v213, %[vk], %[r28]\n\t" \
    "v_cndmask_b32_e64 v214, v214, %[vk], %[r28]\n\t" \
    "v_cndmask_b32_e64 v215, v215, %[vk], %[r28]\n\t" \
    "s_nop 3\n\t" \
    "v_cvt_pk_bf16_f32 v216, v32, v33\n\t" \
    "v_cvt_pk_bf16_f32 v217, v34, v35\n\t" \
    "v_cvt_pk_bf16_f32 v218, v36, v37\n\t" \
    "v_cvt_pk_bf16_f32 v219, v38, v39\n\t" \
    "v_cvt_pk_bf16_f32 v220, v40, v41\n\t" \
    "v_cvt_pk_bf16_f32 v221, v42, v43\n\t" \
    "v_cvt_pk_bf16_f32 v222, v44, v45\n\t" \
    "v_cvt_pk_bf16_f32 v223, v46, v47\n\t" \
    "v_cndmask_b32_e64 v216, v216, %[vk], %[r28]\n\t" \
    "v_cndmask_b32_e64 v217, v217, %[vk], %[r28]\n\t" \
    "v_cndmask_b32_e64 v218, v218, %[vk], %[r28]\n\t" \
    "v_cndmask_b32_e64 v219, v219, %[vk], %[r28]\n\t" \
    "v_cndmask_b32_e64 v220, v220, %[vk], %[r28]\n\t" \
    "v_cndmask_b32_e64 v221, v221, %[vk], %[r28]\n\t" \
    "v_cndmask_b32_e64 v222, v222, %[vk], %[r28]\n\t" \
    "v_cndmask_b32_e64 v223, v223, %[vk], %[r28]\n\t" \
    ""
#define ATTN_B_LOOP \
    "1:\n\t" \
    "s_waitcnt lgkmcnt(2)\n\t" \
    "v_mfma_f32_32x32x16_bf16 v[16:31], v[56:59], v[48:51], 0\n\t" \
    "v_exp_f32 v0, v0\n\t" \
    "v_exp_f32 v1, v1\n\t" \
    "v_exp_f32 v2, v2\n\t" \
    "v_exp_f32 v3, v3\n\t" \
    "s_waitcnt lgkmcnt(1)\n\t" \
    "v_mfma_f32_32x32x16_bf16 v[32:47], v[64:67], v[84:87], v[32:47]\n\t" \
    "v_exp_f32 v4, v4\n\t" \
    "v_exp_f32 v5, v5\n\t" \
    "v_exp_f32 v6, v6\n\t" \
    "v_exp_f32 v7, v7\n\t" \
    "v_cvt_pk_bf16_f32 v72, v0, v1\n\t" \
    "v_cvt_pk_bf16_f32 v73, v2, v3\n\t" \
    "v_mfma_f32_32x32x16_bf16 v[16:31], v[60:63], v[52:55], v[16:31]\n\t" \
    "v_exp_f32 v8, v8\n\t" \
    "v_exp_f32 v9, v9\n\t" \
    "v_exp_f32 v10, v10\n\t" \
    "v_exp_f32 v11, v11\n\t" \
    "v_cvt_pk_bf16_f32 v74, v4, v5\n\t" \
    "v_cvt_pk_bf16_f32 v75, v6, v7\n\t" \
    "s_waitcnt lgkmcnt(0)\n\t" \
    "v_mfma_f32_32x32x16_bf16 v[32:47], v[68:71], v[88:91], v[32:47]\n\t" \
    "v_exp_f32 v12, v12\n\t" \
    "v_exp_f32 v13, v13\n\t" \
    "v_exp_f32 v14, v14\n\t" \
    "v_exp_f32 v15, v15\n\t" \
    "v_cvt_pk_bf16_f32 v76, v8, v9\n\t" \
    "v_cvt_pk_bf16_f32 v77, v10, v11\n\t" \
    "v_cvt_pk_bf16_f32 v78, v12, v13\n\t" \
    "v_cvt_pk_bf16_f32 v79, v14, v15\n\t" \
    "v_mfma_f32_32x32x16_bf16 v[112:127], v[56:59], v[144:147], 0\n\t" \
    "v_exp_f32 v96, v96\n\t" \
    "v_exp_f32 v97, v97\n\t" \
    "v_exp_f32 v98, v98\n\t" \
    "v_exp_f32 v99, v99\n\t" \
    "v_mfma_f32_32x32x16_bf16 v[128:143], v[64:67], v[160:163], v[128:143]\n\t" \
    "v_exp_f32 v100, v100\n\t" \
    "v_exp_f32 v101, v101\n\t" \
    "v_exp_f32 v102, v102\n\t" \
    "v_exp_f32 v103, v103\n\t" \
    "v_cvt_pk_bf16_f32 v152, v96, v97\n\t" \
    "v_cvt_pk_bf16_f32 v153, v98, v99\n\t" \
    "v_mfma_f32_32x32x16_bf16 v[112:127], v[60:63], v[148:151], v[112:127]\n\t" \
    "ds_read_b128 v[56:59], v80 offset:8192\n\t" \
    "ds_read_b128 v[60:63], v80 offset:9216\n\t" \
    "v_exp_f32 v104, v104\n\t" \
    "v_exp_f32 v105, v105\n\t" \
    "v_exp_f32 v106, v106\n\t" \
    "v_exp_f32 v107, v107\n\t" \
    "v_cvt_pk_bf16_f32 v154, v100, v101\n\t" \
    "v_cvt_pk_bf16_f32 v155, v102, v103\n\t" \
    "v_mfma_f32_32x32x16_bf16 v[128:143], v[68:71], v[164:167], v[128:143]\n\t" \
    "ds_read_b128 v[64:67], v80 offset:2048\n\t" \
    "ds_read_b128 v[68:71], v80 offset:3072\n\t" \
    "v_exp_f32 v108, v108\n\t" \
    "v_exp_f32 v109, v109\n\t" \
    "v_exp_f32 v110, v110\n\t" \
    "v_exp_f32 v111, v111\n\t" \
    "v_cvt_pk_bf16_f32 v156, v104, v105\n\t" \
    "v_cvt_pk_bf16_f32 v157, v106, v107\n\t" \
    "v_cvt_pk_bf16_f32 v158, v108, v109\n\t" \
    "v_cvt_pk_bf16_f32 v159, v110, v111\n\t" \
    "s_waitcnt lgkmcnt(2)\n\t" \
    "v_mfma_f32_32x32x16_bf16 v[0:15], v[56:59], v[48:51], 0\n\t" \
    "v_exp_f32 v16, v16\n\t" \
    "v_exp_f32 v17, v17\n\t" \
    "v_exp_f32 v18, v18\n\t" \
    "v_exp_f32 v19, v19\n\t" \
    "s_waitcnt lgkmcnt(1)\n\t" \
    "v_mfma_f32_32x32x16_bf16 v[32:47], v[64:67], v[72:75], v[32:47]\n\t" \
    "v_exp_f32 v20, v20\n\t" \
    "v_exp_f32 v21, v21\n\t" \
    "v_exp_f32 v22, v22\n\t" \
    "v_exp_f32 v23, v23\n\t" \
    "v_cvt_pk_bf16_f32 v84, v16, v17\n\t" \
    "v_cvt_pk_bf16_f32 v85, v18, v19\n\t" \
    "v_mfma_f32_32x32x16_bf16 v[0:15], v[60:63], v[52:55], v[0:15]\n\t" \
    "v_exp_f32 v24, v24\n\t" \
    "v_exp_f32 v25, v25\n\t" \
    "v_exp_f32 v26, v26\n\t" \
    "v_exp_f32 v27, v27\n\t" \
    "v_cvt_pk_bf16_f32 v86, v20, v21\n\t" \
    "v_cvt_pk_bf16_f32 v87, v22, v23\n\t" \
    "s_waitcnt lgkmcnt(0)\n\t" \
    "v_mfma_f32_32x32x16_bf16 v[32:47], v[68:71], v[76:79], v[32:47]\n\t" \
    "v_exp_f32 v28, v28\n\t" \
    "v_exp_f32 v29, v29\n\t" \
    "v_exp_f32 v30, v30\n\t" \
    "v_exp_f32 v31, v31\n\t" \
    "v_cvt_pk_bf16_f32 v88, v24, v25\n\t" \
    "v_cvt_pk_bf16_f32 v89, v26, v27\n\t" \
    "v_cvt_pk_bf16_f32 v90, v28, v29\n\t" \
    "v_cvt_pk_bf16_f32 v91, v30, v31\n\t" \
    "v_mfma_f32_32x32x16_bf16 v[96:111], v[56:59], v[144:147], 0\n\t" \
    "v_exp_f32 v112, v112\n\t" \
    "v_exp_f32 v113, v113\n\t" \
    "v_exp_f32 v114, v114\n\t" \
    "v_exp_f32 v115, v115\n\t" \
    "v_mfma_f32_32x32x16_bf16 v[128:143], v[64:67], v[152:155], v[128:143]\n\t" \
    "v_exp_f32 v116, v116\n\t" \
    "v_exp_f32 v117, v117\n\t" \
    "v_exp_f32 v118, v118\n\t" \
    "v_exp_f32 v119, v119\n\t" \
    "v_cvt_pk_bf16_f32 v160, v112, v113\n\t" \
    "v_cvt_pk_bf16_f32 v161, v114, v115\n\t" \
    "v_mfma_f32_32x32x16_bf16 v[96:111], v[60:63], v[148:151], v[96:111]\n\t" \
    "ds_read_b128 v[56:59], v80 offset:12288\n\t" \
    "ds_read_b128 v[60:63], v80 offset:13312\n\t" \
    "v_exp_f32 v120, v120\n\t" \
    "v_exp_f32 v121, v121\n\t" \
    "v_exp_f32 v122, v122\n\t" \
    "v_exp_f32 v123, v123\n\t" \
    "v_cvt_pk_bf16_f32 v162, v116, v117\n\t" \
    "v_cvt_pk_bf16_f32 v163, v118, v119\n\t" \
    "v_mfma_f32_32x32x16_bf16 v[128:143], v[68:71], v[156:159], v[128:143]\n\t" \
    "ds_read_b128 v[64:67], v80 offset:6144\n\t" \
    "ds_read_b128 v[68:71], v80 offset:7168\n\t" \
    "v_exp_f32 v124, v124\n\t" \
    "v_exp_f32 v125, v125\n\t" \
    "v_exp_f32 v126, v126\n\t" \
    "v_exp_f32 v127, v127\n\t" \
    "v_cvt_pk_bf16_f32 v164, v120, v121\n\t" \
    "v_cvt_pk_bf16_f32 v165, v122, v123\n\t" \
    "v_cvt_pk_bf16_f32 v166, v124, v125\n\t" \
    "v_cvt_pk_bf16_f32 v167, v126, v127\n\t" \
    "s_cmp_eq_u32 %[cnt], 1\n\t" \
    "s_cbranch_scc1 5f\n\t" \
    "s_barrier\n\t" \
    "5:\n\t" \
    "s_waitcnt lgkmcnt(2)\n\t" \
    "v_mfma_f32_32x32x16_bf16 v[16:31], v[56:59], v[48:51], 0\n\t" \
    "v_exp_f32 v0, v0\n\t" \
    "v_exp_f32 v1, v1\n\t" \
    "v_exp_f32 v2, v2\n\t" \
    "v_exp_f32 v3, v3\n\t" \
    "s_waitcnt lgkmcnt(1)\n\t" \
    "v_mfma_f32_32x32x16_bf16 v[32:47], v[64:67], v[84:87], v[32:47]\n\t" \
    "v_exp_f32 v4, v4\n\t" \
    "v_exp_f32 v5, v5\n\t" \
    "v_exp_f32 v6, v6\n\t" \
    "v_exp_f32 v7, v7\n\t" \
    "v_cvt_pk_bf16_f32 v72, v0, v1\n\t" \
    "v_cvt_pk_bf16_f32 v73, v2, v3\n\t" \
    "v_mfma_f32_32x32x16_bf16 v[16:31], v[60:63], v[52:55], v[16:31]\n\t" \
    "v_exp_f32 v8, v8\n\t" \
    "v_exp_f32 v9, v9\n\t" \
    "v_exp_f32 v10, v10\n\t" \
    "v_exp_f32 v11, v11\n\t" \
    "v_cvt_pk_bf16_f32 v74, v4, v5\n\t" \
    "v_cvt_pk_bf16_f32 v75, v6, v7\n\t" \
    "s_waitcnt lgkmcnt(0)\n\t" \
    "v_mfma_f32_32x32x16_bf16 v[32:47], v[68:71], v[88:91], v[32:47]\n\t" \
    "v_exp_f32 v12, v12\n\t" \
    "v_exp_f32 v13, v13\n\t" \
    "v_exp_f32 v14, v14\n\t" \
    "v_exp_f32 v15, v15\n\t" \
    "v_cvt_pk_bf16_f32 v76, v8, v9\n\t" \
    "v_cvt_pk_bf16_f32 v77, v10, v11\n\t" \
    "v_cvt_pk_bf16_f32 v78, v12, v13\n\t" \
    "v_cvt_pk_bf16_f32 v79, v14, v15\n\t" \
    "v_mfma_f32_32x32x16_bf16 v[112:127], v[56:59], v[144:147], 0\n\t" \
    "v_exp_f32 v96, v96\n\t" \
    "v_exp_f32 v97, v97\n\t" \
    "v_exp_f32 v98, v98\n\t" \
    "v_exp_f32 v99, v99\n\t" \
    "v_mfma_f32_32x32x16_bf16 v[128:143], v[64:67], v[160:163], v[128:143]\n\t" \
    "v_exp_f32 v100, v100\n\t" \
    "v_exp_f32 v101, v101\n\t" \
    "v_exp_f32 v102, v102\n\t" \
    "v_exp_f32 v103, v103\n\t" \
    "v_cvt_pk_bf16_f32 v152, v96, v97\n\t" \
    "v_cvt_pk_bf16_f32 v153, v98, v99\n\t" \
    "v_mfma_f32_32x32x16_bf16 v[112:127], v[60:63], v[148:151], v[112:127]\n\t" \
    "ds_read_b128 v[56:59], v81 offset:0\n\t" \
    "ds_read_b128 v[60:63], v81 offset:1024\n\t" \
    "v_exp_f32 v104, v104\n\t" \
    "v_exp_f32 v105, v105\n\t" \
    "v_exp_f32 v106, v106\n\t" \
    "v_exp_f32 v107, v107\n\t" \
    "v_cvt_pk_bf16_f32 v154, v100, v101\n\t" \
    "v_cvt_pk_bf16_f32 v155, v102, v103\n\t" \
    "v_mfma_f32_32x32x16_bf16 v[128:143], v[68:71], v[164:167], v[128:143]\n\t" \
    "ds_read_b128 v[64:67], v80 offset:10240\n\t" \
    "ds_read_b128 v[68:71], v80 offset:11264\n\t" \
    "v_exp_f32 v108, v108\n\t" \
    "v_exp_f32 v109, v109\n\t" \
    "v_exp_f32 v110, v110\n\t" \
    "v_exp_f32 v111, v111\n\t" \
    "v_cvt_pk_bf16_f32 v156, v104, v105\n\t" \
    "v_cvt_pk_bf16_f32 v157, v106, v107\n\t" \
    "v_cvt_pk_bf16_f32 v158, v108, v109\n\t" \
    "v_cvt_pk_bf16_f32 v159, v110, v111\n\t" \
    "s_waitcnt lgkmcnt(2)\n\t" \
    "v_mfma_f32_32x32x16_bf16 v[0:15], v[56:59], v[48:51], 0\n\t" \
    "v_exp_f32 v16, v16\n\t" \
    "v_exp_f32 v17, v17\n\t" \
    "v_exp_f32 v18, v18\n\t" \
    "v_exp_f32 v19, v19\n\t" \
    "s_waitcnt lgkmcnt(1)\n\t" \
    "v_mfma_f32_32x32x16_bf16 v[32:47], v[64:67], v[72:75], v[32:47]\n\t" \
    "v_exp_f32 v20, v20\n\t" \
    "v_exp_f32 v21, v21\n\t" \
    "v_exp_f32 v22, v22\n\t" \
    "v_exp_f32 v23, v23\n\t" \
    "v_cvt_pk_bf16_f32 v84, v16, v17\n\t" \
    "v_cvt_pk_bf16_f32 v85, v18, v19\n\t" \
    "v_mfma_f32_32x32x16_bf16 v[0:15], v[60:63], v[52:55], v[0:15]\n\t" \
    "v_exp_f32 v24, v24\n\t" \
    "v_exp_f32 v25, v25\n\t" \
    "v_exp_f32 v26, v26\n\t" \
    "v_exp_f32 v27, v27\n\t" \
    "v_cvt_pk_bf16_f32 v86, v20, v21\n\t" \
    "v_cvt_pk_bf16_f32 v87, v22, v23\n\t" \
    "s_waitcnt lgkmcnt(0)\n\t" \
    "v_mfma_f32_32x32x16_bf16 v[32:47], v[68:71], v[76:79], v[32:47]\n\t" \
    "v_exp_f32 v28, v28\n\t" \
    "v_exp_f32 v29, v29\n\t" \
    "v_exp_f32 v30, v30\n\t" \
    "v_exp_f32 v31, v31\n\t" \
    "v_cvt_pk_bf16_f32 v88, v24, v25\n\t" \
    "v_cvt_pk_bf16_f32 v89, v26, v27\n\t" \
    "v_cvt_pk_bf16_f32 v90, v28, v29\n\t" \
    "v_cvt_pk_bf16_f32 v91, v30, v31\n\t" \
    "v_mfma_f32_32x32x16_bf16 v[96:111], v[56:59], v[144:147], 0\n\t" \
    "v_exp_f32 v112, v112\n\t" \
    "v_exp_f32 v113, v113\n\t" \
    "v_exp_f32 v114, v114\n\t" \
    "v_exp_f32 v115, v115\n\t" \
    "v_mfma_f32_32x32x16_bf16 v[128:143], v[64:67], v[152:155], v[128:143]\n\t" \
    "v_exp_f32 v116, v116\n\t" \
    "v_exp_f32 v117, v117\n\t" \
    "v_exp_f32 v118, v118\n\t" \
    "v_exp_f32 v119, v119\n\t" \
    "v_cvt_pk_bf16_f32 v160, v112, v113\n\t" \
    "v_cvt_pk_bf16_f32 v161, v114, v115\n\t" \
    "v_mfma_f32_32x32x16_bf16 v[96:111], v[60:63], v[148:151], v[96:111]\n\t" \
    "ds_read_b128 v[56:59], v81 offset:4096\n\t" \
    "ds_read_b128 v[60:63], v81 offset:5120\n\t" \
    "v_exp_f32 v120, v120\n\t" \
    "v_exp_f32 v121, v121\n\t" \
    "v_exp_f32 v122, v122\n\t" \
    "v_exp_f32 v123, v123\n\t" \
    "v_cvt_pk_bf16_f32 v162, v116, v117\n\t" \
    "v_cvt_pk_bf16_f32 v163, v118, v119\n\t" \
    "v_mfma_f32_32x32x16_bf16 v[128:143], v[68:71], v[156:159], v[128:143]\n\t" \
    "ds_read_b128 v[64:67], v80 offset:14336\n\t" \
    "ds_read_b128 v[68:71], v80 offset:15360\n\t" \
    "s_add_u32 %[slot], %[slot], 1\n\t" \
    "s_cmp_eq_u32 %[slot], 3\n\t" \
    "s_mov_b32 %[dl], 32768\n\t" \
    "s_cselect_b32 %[dl], 0xffff0000, %[dl]\n\t" \
    "s_cselect_b32 %[slot], 0, %[slot]\n\t" \
    "v_mov_b32 v80, v81\n\t" \
    "v_add_u32 v81, %[dl], v81\n\t" \
    "v_exp_f32 v124, v124\n\t" \
    "v_exp_f32 v125, v125\n\t" \
    "v_exp_f32 v126, v126\n\t" \
    "v_exp_f32 v127, v127\n\t" \
    "v_cvt_pk_bf16_f32 v164, v120, v121\n\t" \
    "v_cvt_pk_bf16_f32 v165, v122, v123\n\t" \
    "v_cvt_pk_bf16_f32 v166, v124, v125\n\t" \
    "v_cvt_pk_bf16_f32 v167, v126, v127\n\t" \
    "s_sub_u32 %[cnt], %[cnt], 1\n\t" \
    "s_cmp_lg_u32 %[cnt], 0\n\t" \
    "s_cbranch_scc1 1b\n\t" \
    "s_waitcnt lgkmcnt(0)\n\t" \
    "s_nop 0\n\t" \
    "v_mfma_f32_32x32x16_bf16 v[32:47], v[64:67], v[84:87], v[32:47]\n\t" \
    "v_mfma_f32_32x32x16_bf16 v[128:143], v[64:67], v[160:163], v[128:143]\n\t" \
    "v_mfma_f32_32x32x16_bf16 v[32:47], v[68:71], v[88:91], v[32:47]\n\t" \
    "v_mfma_f32_32x32x16_bf16 v[128:143], v[68:71], v[164:167], v[128:143]\n\t" \
    "s_nop 7\n\t" \
    "s_nop 7\n\t" \
    "s_cmp_eq_u32 %[kh], 0\n\t" \
    "s_cbranch_scc1 8f\n\t" \
    "ds_write_b128 %[pa], v[32:35] offset:0\n\t" \
    "ds_write_b128 %[pa], v[36:39] offset:1024\n\t" \
    "ds_write_b128 %[pa], v[40:43] offset:2048\n\t" \
    "ds_write_b128 %[pa], v[44:47] offset:3072\n\t" \
    "ds_write_b32 %[pm], %[mX]\n\t" \
    "s_branch 9f\n\t" \
    "8:\n\t" \
    "ds_write_b128 %[pa], v[128:131] offset:0\n\t" \
    "ds_write_b128 %[pa], v[132:135] offset:1024\n\t" \
    "ds_write_b128 %[pa], v[136:139] offset:2048\n\t" \
    "ds_write_b128 %[pa], v[140:143] offset:3072\n\t" \
    "ds_write_b32 %[pm], %[mY]\n\t" \
    "9:\n\t" \
    "v_mov_b32 v0, 1\n\t" \
    "s_waitcnt lgkmcnt(0)\n\t" \
    "s_mov_b64 exec, 1\n\t" \
    "ds_write_b32 %[cm], v0\n\t" \
    "s_mov_b64 exec, -1\n\t" \
    "s_mov_b32 %[dl], 0x10000\n\t" \
    "10:\n\t" \
    "ds_read_b32 v1, %[cp]\n\t" \
    "s_waitcnt lgkmcnt(0)\n\t" \
    "v_readfirstlane_b32 %[slot], v1\n\t" \
    "s_sub_u32 %[dl], %[dl], 1\n\t" \
    "s_cbranch_scc1 11f\n\t" \
    "s_cmp_eq_u32 %[slot], 0\n\t" \
    "s_cbranch_scc1 10b\n\t" \
    "11:\n\t" \
    ""
#define ATTN_A0 \
    "ds_read_b128 v[56:59], v80 offset:0\n\t" \
    "ds_read_b128 v[60:63], v80 offset:1024\n\t" \
    "ds_read_b128 v[64:67], v80 offset:2048\n\t" \
    "ds_read_b128 v[68:71], v80 offset:3072\n\t" \
    "ds_read_b128 v[184:187], v80 offset:4096\n\t" \
    "ds_read_b128 v[188:191], v80 offset:5120\n\t" \
    "s_waitcnt lgkmcnt(4)\n\t" \
    "v_mfma_f32_32x32x16_bf16 v[0:15], v[56:59], v[48:51], v[168:183]\n\t" \
    "v_mfma_f32_32x32x16_bf16 v[0:15], v[60:63], v[52:55], v[0:15]\n\t" \
    "s_waitcnt lgkmcnt(0)\n\t" \
    "s_nop 7\n\t" \
    "s_nop 3\n\t" \
    "v_mfma_f32_32x32x16_bf16 v[16:31], v[184:187], v[48:51], 0\n\t" \
    "v_exp_f32 v0, v0\n\t" \
    "v_exp_f32 v1, v1\n\t" \
    "v_exp_f32 v2, v2\n\t" \
    "v_exp_f32 v3, v3\n\t" \
    "v_exp_f32 v4, v4\n\t" \
    "v_exp_f32 v5, v5\n\t" \
    "v_exp_f32 v6, v6\n\t" \
    "v_exp_f32 v7, v7\n\t" \
    "v_cvt_pk_bf16_f32 v72, v0, v1\n\t" \
    "v_cvt_pk_bf16_f32 v73, v2, v3\n\t" \
    "v_mfma_f32_32x32x16_bf16 v[16:31], v[188:191], v[52:55], v[16:31]\n\t" \
    "ds_read_b128 v[56:59], v80 offset:8192\n\t" \
    "ds_read_b128 v[60:63], v80 offset:9216\n\t" \
    "v_exp_f32 v8, v8\n\t" \
    "v_exp_f32 v9, v9\n\t" \
    "v_exp_f32 v10, v10\n\t" \
    "v_exp_f32 v11, v11\n\t" \
    "v_cvt_pk_bf16_f32 v74, v4, v5\n\t" \
    "v_cvt_pk_bf16_f32 v75, v6, v7\n\t" \
    "ds_read_b128 v[192:195], v80 offset:6144\n\t" \
    "ds_read_b128 v[196:199], v80 offset:7168\n\t" \
    "v_exp_f32 v12, v12\n\t" \
    "v_exp_f32 v13, v13\n\t" \
    "v_exp_f32 v14, v14\n\t" \
    "v_exp_f32 v15, v15\n\t" \
    "v_cvt_pk_bf16_f32 v76, v8, v9\n\t" \
    "v_cvt_pk_bf16_f32 v77, v10, v11\n\t" \
    "v_cvt_pk_bf16_f32 v78, v12, v13\n\t" \
    "v_cvt_pk_bf16_f32 v79, v14, v15\n\t" \
    "s_waitcnt lgkmcnt(0)\n\t" \
    "v_mfma_f32_32x32x16_bf16 v[96:111], v[184:187], v[144:147], v[168:183]\n\t" \
    "v_exp_f32 v16, v16\n\t" \
    "v_exp_f32 v17, v17\n\t" \
    "v_exp_f32 v18, v18\n\t" \
    "v_exp_f32 v19, v19\n\t" \
    "v_mfma_f32_32x32x16_bf16 v[32:47], v[64:67], v[72:75], 0\n\t" \
    "v_exp_f32 v20, v20\n\t" \
    "v_exp_f32 v21, v21\n\t" \
    "v_exp_f32 v22, v22\n\t" \
    "v_exp_f32 v23, v23\n\t" \
    "v_cvt_pk_bf16_f32 v84, v16, v17\n\t" \
    "v_cvt_pk_bf16_f32 v85, v18, v19\n\t" \
    "v_mfma_f32_32x32x16_bf16 v[96:111], v[188:191], v[148:151], v[96:111]\n\t" \
    "v_exp_f32 v24, v24\n\t" \
    "v_exp_f32 v25, v25\n\t" \
    "v_exp_f32 v26, v26\n\t" \
    "v_exp_f32 v27, v27\n\t" \
    "v_cvt_pk_bf16_f32 v86, v20, v21\n\t" \
    "v_cvt_pk_bf16_f32 v87, v22, v23\n\t" \
    "v_mfma_f32_32x32x16_bf16 v[32:47], v[68:71], v[76:79], v[32:47]\n\t" \
    "v_exp_f32 v28, v28\n\t" \
    "v_exp_f32 v29, v29\n\t" \
    "v_exp_f32 v30, v30\n\t" \
    "v_exp_f32 v31, v31\n\t" \
    "v_cvt_pk_bf16_f32 v88, v24, v25\n\t" \
    "v_cvt_pk_bf16_f32 v89, v26, v27\n\t" \
    "v_cvt_pk_bf16_f32 v90, v28, v29\n\t" \
    "v_cvt_pk_bf16_f32 v91, v30, v31\n\t" \
    "s_waitcnt lgkmcnt(0)\n\t" \
    "v_mfma_f32_32x32x16_bf16 v[0:15], v[56:59], v[48:51], 0\n\t" \
    "v_exp_f32 v96, v96\n\t" \
    "v_exp_f32 v97, v97\n\t" \
    "v_exp_f32 v98, v98\n\t" \
    "v_exp_f32 v99, v99\n\t" \
    "v_mfma_f32_32x32x16_bf16 v[32:47], v[192:195], v[84:87], v[32:47]\n\t" \
    "v_exp_f32 v100, v100\n\t" \
    "v_exp_f32 v101, v101\n\t" \
    "v_exp_f32 v102, v102\n\t" \
    "v_exp_f32 v103, v103\n\t" \
    "v_cvt_pk_bf16_f32 v152, v96, v97\n\t" \
    "v_cvt_pk_bf16_f32 v153, v98, v99\n\t" \
    "v_mfma_f32_32x32x16_bf16 v[0:15], v[60:63], v[52:55], v[0:15]\n\t" \
    "ds_read_b128 v[184:187], v80 offset:12288\n\t" \
    "ds_read_b128 v[188:191], v80 offset:13312\n\t" \
    "v_exp_f32 v104, v104\n\t" \
    "v_exp_f32 v105, v105\n\t" \
    "v_exp_f32 v106, v106\n\t" \
    "v_exp_f32 v107, v107\n\t" \
    "v_cvt_pk_bf16_f32 v154, v100, v101\n\t" \
    "v_cvt_pk_bf16_f32 v155, v102, v103\n\t" \
    "v_mfma_f32_32x32x16_bf16 v[32:47], v[196:199], v[88:91], v[32:47]\n\t" \
    "ds_read_b128 v[64:67], v80 offset:10240\n\t" \
    "ds_read_b128 v[68:71], v80 offset:11264\n\t" \
    "v_exp_f32 v108, v108\n\t" \
    "v_exp_f32 v109, v109\n\t" \
    "v_exp_f32 v110, v110\n\t" \
    "v_exp_f32 v111, v111\n\t" \
    "v_cvt_pk_bf16_f32 v156, v104, v105\n\t" \
    "v_cvt_pk_bf16_f32 v157, v106, v107\n\t" \
    "v_cvt_pk_bf16_f32 v158, v108, v109\n\t" \
    "v_cvt_pk_bf16_f32 v159, v110, v111\n\t" \
    "s_waitcnt lgkmcnt(0)\n\t" \
    "v_mfma_f32_32x32x16_bf16 v[112:127], v[56:59], v[144:147], 0\n\t" \
    "v_exp_f32 v0, v0\n\t" \
    "v_exp_f32 v1, v1\n\t" \
    "v_exp_f32 v2, v2\n\t" \
    "v_exp_f32 v3, v3\n\t" \
    "v_mfma_f32_32x32x16_bf16 v[128:143], v[192:195], v[152:155], 0\n\t" \
    "v_exp_f32 v4, v4\n\t" \
    "v_exp_f32 v5, v5\n\t" \
    "v_exp_f32 v6, v6\n\t" \
    "v_exp_f32 v7, v7\n\t" \
    "v_cvt_pk_bf16_f32 v72, v0, v1\n\t" \
    "v_cvt_pk_bf16_f32 v73, v2, v3\n\t" \
    "v_mfma_f32_32x32x16_bf16 v[112:127], v[60:63], v[148:151], v[112:127]\n\t" \
    "v_exp_f32 v8, v8\n\t" \
    "v_exp_f32 v9, v9\n\t" \
    "v_exp_f32 v10, v10\n\t" \
    "v_exp_f32 v11, v11\n\t" \
    "v_cvt_pk_bf16_f32 v74, v4, v5\n\t" \
    "v_cvt_pk_bf16_f32 v75, v6, v7\n\t" \
    "v_mfma_f32_32x32x16_bf16 v[128:143], v[196:199], v[156:159], v[128:143]\n\t" \
    "v_exp_f32 v12, v12\n\t" \
    "v_exp_f32 v13, v13\n\t" \
    "v_exp_f32 v14, v14\n\t" \
    "v_exp_f32 v15, v15\n\t" \
    "v_cvt_pk_bf16_f32 v76, v8, v9\n\t" \
    "v_cvt_pk_bf16_f32 v77, v10, v11\n\t" \
    "v_cvt_pk_bf16_f32 v78, v12, v13\n\t" \
    "v_cvt_pk_bf16_f32 v79, v14, v15\n\t" \
    "s_waitcnt lgkmcnt(0)\n\t" \
    "v_mfma_f32_32x32x16_bf16 v[16:31], v[184:187], v[48:51], 0\n\t" \
    "v_exp_f32 v112, v112\n\t" \
    "v_exp_f32 v113, v113\n\t" \
    "v_exp_f32 v114, v114\n\t" \
    "v_exp_f32 v115, v115\n\t" \
    "v_mfma_f32_32x32x16_bf16 v[32:47], v[64:67], v[72:75], v[32:47]\n\t" \
    "v_exp_f32 v116, v116\n\t" \
    "v_exp_f32 v117, v117\n\t" \
    "v_exp_f32 v118, v118\n\t" \
    "v_exp_f32 v119, v119\n\t" \
    "v_cvt_pk_bf16_f32 v160, v112, v113\n\t" \
    "v_cvt_pk_bf16_f32 v161, v114, v115\n\t" \
    "v_mfma_f32_32x32x16_bf16 v[16:31], v[188:191], v[52:55], v[16:31]\n\t" \
    "ds_read_b128 v[56:59], v80 offset:16384\n\t" \
    "ds_read_b128 v[60:63], v80 offset:17408\n\t" \
    "v_exp_f32 v120, v120\n\t" \
    "v_exp_f32 v121, v121\n\t" \
    "v_exp_f32 v122, v122\n\t" \
    "v_exp_f32 v123, v123\n\t" \
    "v_cvt_pk_bf16_f32 v162, v116, v117\n\t" \
    "v_cvt_pk_bf16_f32 v163, v118, v119\n\t" \
    "v_mfma_f32_32x32x16_bf16 v[32:47], v[68:71], v[76:79], v[32:47]\n\t" \
    "ds_read_b128 v[192:195], v80 offset:14336\n\t" \
    "ds_read_b128 v[196:199], v80 offset:15360\n\t" \
    "v_exp_f32 v124, v124\n\t" \
    "v_exp_f32 v125, v125\n\t" \
    "v_exp_f32 v126, v126\n\t" \
    "v_exp_f32 v127, v127\n\t" \
    "v_cvt_pk_bf16_f32 v164, v120, v121\n\t" \
    "v_cvt_pk_bf16_f32 v165, v122, v123\n\t" \
    "v_cvt_pk_bf16_f32 v166, v124, v125\n\t" \
    "v_cvt_pk_bf16_f32 v167, v126, v127\n\t" \
    "s_waitcnt lgkmcnt(0)\n\t" \
    "v_mfma_f32_32x32x16_bf16 v[96:111], v[184:187], v[144:147], 0\n\t" \
    "v_exp_f32 v16, v16\n\t" \
    "v_exp_f32 v17, v17\n\t" \
    "v_exp_f32 v18, v18\n\t" \
    "v_exp_f32 v19, v19\n\t" \
    "v_mfma_f32_32x32x16_bf16 v[128:143], v[64:67], v[160:163], v[128:143]\n\t" \
    "v_exp_f32 v20, v20\n\t" \
    "v_exp_f32 v21, v21\n\t" \
    "v_exp_f32 v22, v22\n\t" \
    "v_exp_f32 v23, v23\n\t" \
    "v_cvt_pk_bf16_f32 v84, v16, v17\n\t" \
    "v_cvt_pk_bf16_f32 v85, v18, v19\n\t" \
    "v_mfma_f32_32x32x16_bf16 v[96:111], v[188:191], v[148:151], v[96:111]\n\t" \
    "v_exp_f32 v24, v24\n\t" \
    "v_exp_f32 v25, v25\n\t" \
    "v_exp_f32 v26, v26\n\t" \
    "v_exp_f32 v27, v27\n\t" \
    "v_cvt_pk_bf16_f32 v86, v20, v21\n\t" \
    "v_cvt_pk_bf16_f32 v87, v22, v23\n\t" \
    "v_mfma_f32_32x32x16_bf16 v[128:143], v[68:71], v[164:167], v[128:143]\n\t" \
    "v_exp_f32 v28, v28\n\t" \
    "v_exp_f32 v29, v29\n\t" \
    "v_exp_f32 v30, v30\n\t" \
    "v_exp_f32 v31, v31\n\t" \
    "v_cvt_pk_bf16_f32 v88, v24, v25\n\t" \
    "v_cvt_pk_bf16_f32 v89, v26, v27\n\t" \
    "v_cvt_pk_bf16_f32 v90, v28, v29\n\t" \
    "v_cvt_pk_bf16_f32 v91, v30, v31\n\t" \
    "s_waitcnt lgkmcnt(0)\n\t" \
    "v_mfma_f32_32x32x16_bf16 v[0:15], v[56:59], v[48:51], 0\n\t" \
    "v_exp_f32 v96, v96\n\t" \
    "v_exp_f32 v97, v97\n\t" \
    "v_exp_f32 v98, v98\n\t" \
    "v_exp_f32 v99, v99\n\t" \
    "v_mfma_f32_32x32x16_bf16 v[32:47], v[192:195], v[84:87], v[32:47]\n\t" \
    "v_exp_f32 v100, v100\n\t" \
    "v_exp_f32 v101, v101\n\t" \
    "v_exp_f32 v102, v102\n\t" \
    "v_exp_f32 v103, v103\n\t" \
    "v_cvt_pk_bf16_f32 v152, v96, v97\n\t" \
    "v_cvt_pk_bf16_f32 v153, v98, v99\n\t" \
    "v_mfma_f32_32x32x16_bf16 v[0:15], v[60:63], v[52:55], v[0:15]\n\t" \
    "v_exp_f32 v104, v104\n\t" \
    "v_exp_f32 v105, v105\n\t" \
    "v_exp_f32 v106, v106\n\t" \
    "v_exp_f32 v107, v107\n\t" \
    "v_cvt_pk_bf16_f32 v154, v100, v101\n\t" \
    "v_cvt_pk_bf16_f32 v155, v102, v103\n\t" \
    "v_mfma_f32_32x32x16_bf16 v[32:47], v[196:199], v[88:91], v[32:47]\n\t" \
    "ds_read_b128 v[64:67], v80 offset:18432\n\t" \
    "ds_read_b128 v[68:71], v80 offset:19456\n\t" \
    "v_exp_f32 v108, v108\n\t" \
    "v_exp_f32 v109, v109\n\t" \
    "v_exp_f32 v110, v110\n\t" \
    "v_exp_f32 v111, v111\n\t" \
    "v_cvt_pk_bf16_f32 v156, v104, v105\n\t" \
    "v_cvt_pk_bf16_f32 v157, v106, v107\n\t" \
    "v_cvt_pk_bf16_f32 v158, v108, v109\n\t" \
    "v_cvt_pk_bf16_f32 v159, v110, v111\n\t" \
    "s_waitcnt lgkmcnt(0)\n\t" \
    "v_mfma_f32_32x32x16_bf16 v[112:127], v[56:59], v[144:147], 0\n\t" \
    "v_exp_f32 v0, v0\n\t" \
    "v_exp_f32 v1, v1\n\t" \
    "v_exp_f32 v2, v2\n\t" \
    "v_exp_f32 v3, v3\n\t" \
    "v_mfma_f32_32x32x16_bf16 v[128:143], v[192:195], v[152:155], v[128:143]\n\t" \
    "v_exp_f32 v4, v4\n\t" \
    "v_exp_f32 v5, v5\n\t" \
    "v_exp_f32 v6, v6\n\t" \
    "v_exp_f32 v7, v7\n\t" \
    "v_cvt_pk_bf16_f32 v72, v0, v1\n\t" \
    "v_cvt_pk_bf16_f32 v73, v2, v3\n\t" \
    "v_mfma_f32_32x32x16_bf16 v[112:127], v[60:63], v[148:151], v[112:127]\n\t" \
    "v_exp_f32 v8, v8\n\t" \
    "v_exp_f32 v9, v9\n\t" \
    "v_exp_f32 v10, v10\n\t" \
    "v_exp_f32 v11, v11\n\t" \
    "v_cvt_pk_bf16_f32 v74, v4, v5\n\t" \
    "v_cvt_pk_bf16_f32 v75, v6, v7\n\t" \
    "v_mfma_f32_32x32x16_bf16 v[128:143], v[196:199], v[156:159], v[128:143]\n\t" \
    "v_exp_f32 v12, v12\n\t" \
    "v_exp_f32 v13, v13\n\t" \
    "v_exp_f32 v14, v14\n\t" \
    "v_exp_f32 v15, v15\n\t" \
    "v_cvt_pk_bf16_f32 v76, v8, v9\n\t" \
    "v_cvt_pk_bf16_f32 v77, v10, v11\n\t" \
    "v_cvt_pk_bf16_f32 v78, v12, v13\n\t" \
    "v_cvt_pk_bf16_f32 v79, v14, v15\n\t" \
    "s_waitcnt lgkmcnt(0)\n\t" \
    "v_exp_f32 v112, v112\n\t" \
    "v_exp_f32 v113, v113\n\t" \
    "v_exp_f32 v114, v114\n\t" \
    "v_exp_f32 v115, v115\n\t" \
    "v_mfma_f32_32x32x16_bf16 v[32:47], v[64:67], v[72:75], v[32:47]\n\t" \
    "v_exp_f32 v116, v116\n\t" \
    "v_exp_f32 v117, v117\n\t" \
    "v_exp_f32 v118, v118\n\t" \
    "v_exp_f32 v119, v119\n\t" \
    "v_cvt_pk_bf16_f32 v160, v112, v113\n\t" \
    "v_cvt_pk_bf16_f32 v161, v114, v115\n\t" \
    "v_exp_f32 v120, v120\n\t" \
    "v_exp_f32 v121, v121\n\t" \
    "v_exp_f32 v122, v122\n\t" \
    "v_exp_f32 v123, v123\n\t" \
    "v_cvt_pk_bf16_f32 v162, v116, v117\n\t" \
    "v_cvt_pk_bf16_f32 v163, v118, v119\n\t" \
    "v_mfma_f32_32x32x16_bf16 v[32:47], v[68:71], v[76:79], v[32:47]\n\t" \
    "v_exp_f32 v124, v124\n\t" \
    "v_exp_f32 v125, v125\n\t" \
    "v_exp_f32 v126, v126\n\t" \
    "v_exp_f32 v127, v127\n\t" \
    "v_cvt_pk_bf16_f32 v164, v120, v121\n\t" \
    "v_cvt_pk_bf16_f32 v165, v122, v123\n\t" \
    "v_cvt_pk_bf16_f32 v166, v124, v125\n\t" \
    "v_cvt_pk_bf16_f32 v167, v126, v127\n\t" \
    "s_waitcnt lgkmcnt(0)\n\t" \
    "s_nop 0\n\t" \
    "v_mfma_f32_32x32x16_bf16 v[128:143], v[64:67], v[160:163], v[128:143]\n\t" \
    "v_mfma_f32_32x32x16_bf16 v[128:143], v[68:71], v[164:167], v[128:143]\n\t" \
    "s_nop 7\n\t" \
    "s_nop 7\n\t" \
    ""
#define ATTN_A1 \
    "ds_read_b128 v[56:59], v80 offset:0\n\t" \
    "ds_read_b128 v[60:63], v80 offset:1024\n\t" \
    "ds_read_b128 v[64:67], v80 offset:2048\n\t" \
    "ds_read_b128 v[68:71], v80 offset:3072\n\t" \
    "ds_read_b128 v[184:187], v80 offset:4096\n\t" \
    "ds_read_b128 v[188:191], v80 offset:5120\n\t" \
    "s_waitcnt lgkmcnt(4)\n\t" \
    "v_mfma_f32_32x32x16_bf16 v[0:15], v[56:59], v[48:51], 0\n\t" \
    "v_mfma_f32_32x32x16_bf16 v[0:15], v[60:63], v[52:55], v[0:15]\n\t" \
    "s_waitcnt lgkmcnt(0)\n\t" \
    "s_nop 7\n\t" \
    "s_nop 3\n\t" \
    "v_mfma_f32_32x32x16_bf16 v[96:111], v[56:59], v[144:147], 0\n\t" \
    "v_exp_f32 v0, v0\n\t" \
    "v_exp_f32 v1, v1\n\t" \
    "v_exp_f32 v2, v2\n\t" \
    "v_exp_f32 v3, v3\n\t" \
    "v_exp_f32 v4, v4\n\t" \
    "v_exp_f32 v5, v5\n\t" \
    "v_exp_f32 v6, v6\n\t" \
    "v_exp_f32 v7, v7\n\t" \
    "v_cvt_pk_bf16_f32 v72, v0, v1\n\t" \
    "v_cvt_pk_bf16_f32 v73, v2, v3\n\t" \
    "v_mfma_f32_32x32x16_bf16 v[96:111], v[60:63], v[148:151], v[96:111]\n\t" \
    "v_exp_f32 v8, v8\n\t" \
    "v_exp_f32 v9, v9\n\t" \
    "v_exp_f32 v10, v10\n\t" \
    "v_exp_f32 v11, v11\n\t" \
    "v_cvt_pk_bf16_f32 v74, v4, v5\n\t" \
    "v_cvt_pk_bf16_f32 v75, v6, v7\n\t" \
    "v_exp_f32 v12, v12\n\t" \
    "v_exp_f32 v13, v13\n\t" \
    "v_exp_f32 v14, v14\n\t" \
    "v_exp_f32 v15, v15\n\t" \
    "v_cvt_pk_bf16_f32 v76, v8, v9\n\t" \
    "v_cvt_pk_bf16_f32 v77, v10, v11\n\t" \
    "v_cvt_pk_bf16_f32 v78, v12, v13\n\t" \
    "v_cvt_pk_bf16_f32 v79, v14, v15\n\t" \
    "s_waitcnt lgkmcnt(0)\n\t" \
    "v_mfma_f32_32x32x16_bf16 v[16:31], v[184:187], v[48:51], 0\n\t" \
    "v_exp_f32 v96, v96\n\t" \
    "v_exp_f32 v97, v97\n\t" \
    "v_exp_f32 v98, v98\n\t" \
    "v_exp_f32 v99, v99\n\t" \
    "v_mfma_f32_32x32x16_bf16 v[32:47], v[64:67], v[72:75], 0\n\t" \
    "v_exp_f32 v100, v100\n\t" \
    "v_exp_f32 v101, v101\n\t" \
    "v_exp_f32 v102, v102\n\t" \
    "v_exp_f32 v103, v103\n\t" \
    "v_cvt_pk_bf16_f32 v152, v96, v97\n\t" \
    "v_cvt_pk_bf16_f32 v153, v98, v99\n\t" \
    "v_mfma_f32_32x32x16_bf16 v[16:31], v[188:191], v[52:55], v[16:31]\n\t" \
    "ds_read_b128 v[56:59], v80 offset:8192\n\t" \
    "ds_read_b128 v[60:63], v80 offset:9216\n\t" \
    "v_exp_f32 v104, v104\n\t" \
    "v_exp_f32 v105, v105\n\t" \
    "v_exp_f32 v106, v106\n\t" \
    "v_exp_f32 v107, v107\n\t" \
    "v_cvt_pk_bf16_f32 v154, v100, v101\n\t" \
    "v_cvt_pk_bf16_f32 v155, v102, v103\n\t" \
    "v_mfma_f32_32x32x16_bf16 v[32:47], v[68:71], v[76:79], v[32:47]\n\t" \
    "ds_read_b128 v[192:195], v80 offset:6144\n\t" \
    "ds_read_b128 v[196:199], v80 offset:7168\n\t" \
    "v_exp_f32 v108, v108\n\t" \
    "v_exp_f32 v109, v109\n\t" \
    "v_exp_f32 v110, v110\n\t" \
    "v_exp_f32 v111, v111\n\t" \
    "v_cvt_pk_bf16_f32 v156, v104, v105\n\t" \
    "v_cvt_pk_bf16_f32 v157, v106, v107\n\t" \
    "v_cvt_pk_bf16_f32 v158, v108, v109\n\t" \
    "v_cvt_pk_bf16_f32 v159, v110, v111\n\t" \
    "s_waitcnt lgkmcnt(0)\n\t" \
    "v_mfma_f32_32x32x16_bf16 v[112:127], v[184:187], v[144:147], 0\n\t" \
    "v_exp_f32 v16, v16\n\t" \
    "v_exp_f32 v17, v17\n\t" \
    "v_exp_f32 v18, v18\n\t" \
    "v_exp_f32 v19, v19\n\t" \
    "v_mfma_f32_32x32x16_bf16 v[128:143], v[64:67], v[152:155], 0\n\t" \
    "v_exp_f32 v20, v20\n\t" \
    "v_exp_f32 v21, v21\n\t" \
    "v_exp_f32 v22, v22\n\t" \
    "v_exp_f32 v23, v23\n\t" \
    "v_cvt_pk_bf16_f32 v84, v16, v17\n\t" \
    "v_cvt_pk_bf16_f32 v85, v18, v19\n\t" \
    "v_mfma_f32_32x32x16_bf16 v[112:127], v[188:191], v[148:151], v[112:127]\n\t" \
    "v_exp_f32 v24, v24\n\t" \
    "v_exp_f32 v25, v25\n\t" \
    "v_exp_f32 v26, v26\n\t" \
    "v_exp_f32 v27, v27\n\t" \
    "v_cvt_pk_bf16_f32 v86, v20, v21\n\t" \
    "v_cvt_pk_bf16_f32 v87, v22, v23\n\t" \
    "v_mfma_f32_32x32x16_bf16 v[128:143], v[68:71], v[156:159], v[128:143]\n\t" \
    "v_exp_f32 v28, v28\n\t" \
    "v_exp_f32 v29, v29\n\t" \
    "v_exp_f32 v30, v30\n\t" \
    "v_exp_f32 v31, v31\n\t" \
    "v_cvt_pk_bf16_f32 v88, v24, v25\n\t" \
    "v_cvt_pk_bf16_f32 v89, v26, v27\n\t" \
    "v_cvt_pk_bf16_f32 v90, v28, v29\n\t" \
    "v_cvt_pk_bf16_f32 v91, v30, v31\n\t" \
    "s_waitcnt lgkmcnt(0)\n\t" \
    "v_mfma_f32_32x32x16_bf16 v[0:15], v[56:59], v[48:51], 0\n\t" \
    "v_exp_f32 v112, v112\n\t" \
    "v_exp_f32 v113, v113\n\t" \
    "v_exp_f32 v114, v114\n\t" \
    "v_exp_f32 v115, v115\n\t" \
    "v_mfma_f32_32x32x16_bf16 v[32:47], v[192:195], v[84:87], v[32:47]\n\t" \
    "v_exp_f32 v116, v116\n\t" \
    "v_exp_f32 v117, v117\n\t" \
    "v_exp_f32 v118, v118\n\t" \
    "v_exp_f32 v119, v119\n\t" \
    "v_cvt_pk_bf16_f32 v160, v112, v113\n\t" \
    "v_cvt_pk_bf16_f32 v161, v114, v115\n\t" \
    "v_mfma_f32_32x32x16_bf16 v[0:15], v[60:63], v[52:55], v[0:15]\n\t" \
    "ds_read_b128 v[184:187], v80 offset:12288\n\t" \
    "ds_read_b128 v[188:191], v80 offset:13312\n\t" \
    "v_exp_f32 v120, v120\n\t" \
    "v_exp_f32 v121, v121\n\t" \
    "v_exp_f32 v122, v122\n\t" \
    "v_exp_f32 v123, v123\n\t" \
    "v_cvt_pk_bf16_f32 v162, v116, v117\n\t" \
    "v_cvt_pk_bf16_f32 v163, v118, v119\n\t" \
    "v_mfma_f32_32x32x16_bf16 v[32:47], v[196:199], v[88:91], v[32:47]\n\t" \
    "ds_read_b128 v[64:67], v80 offset:10240\n\t" \
    "ds_read_b128 v[68:71], v80 offset:11264\n\t" \
    "v_exp_f32 v124, v124\n\t" \
    "v_exp_f32 v125, v125\n\t" \
    "v_exp_f32 v126, v126\n\t" \
    "v_exp_f32 v127, v127\n\t" \
    "v_cvt_pk_bf16_f32 v164, v120, v121\n\t" \
    "v_cvt_pk_bf16_f32 v165, v122, v123\n\t" \
    "v_cvt_pk_bf16_f32 v166, v124, v125\n\t" \
    "v_cvt_pk_bf16_f32 v167, v126, v127\n\t" \
    "s_waitcnt lgkmcnt(0)\n\t" \
    "v_mfma_f32_32x32x16_bf16 v[96:111], v[56:59], v[144:147], 0\n\t" \
    "v_exp_f32 v0, v0\n\t" \
    "v_exp_f32 v1, v1\n\t" \
    "v_exp_f32 v2, v2\n\t" \
    "v_exp_f32 v3, v3\n\t" \
    "v_mfma_f32_32x32x16_bf16 v[128:143], v[192:195], v[160:163], v[128:143]\n\t" \
    "v_exp_f32 v4, v4\n\t" \
    "v_exp_f32 v5, v5\n\t" \
    "v_exp_f32 v6, v6\n\t" \
    "v_exp_f32 v7, v7\n\t" \
    "v_cvt_pk_bf16_f32 v72, v0, v1\n\t" \
    "v_cvt_pk_bf16_f32 v73, v2, v3\n\t" \
    "v_mfma_f32_32x32x16_bf16 v[96:111], v[60:63], v[148:151], v[96:111]\n\t" \
    "v_exp_f32 v8, v8\n\t" \
    "v_exp_f32 v9, v9\n\t" \
    "v_exp_f32 v10, v10\n\t" \
    "v_exp_f32 v11, v11\n\t" \
    "v_cvt_pk_bf16_f32 v74, v4, v5\n\t" \
    "v_cvt_pk_bf16_f32 v75, v6, v7\n\t" \
    "v_mfma_f32_32x32x16_bf16 v[128:143], v[196:199], v[164:167], v[128:143]\n\t" \
    "v_exp_f32 v12, v12\n\t" \
    "v_exp_f32 v13, v13\n\t" \
    "v_exp_f32 v14, v14\n\t" \
    "v_exp_f32 v15, v15\n\t" \
    "v_cvt_pk_bf16_f32 v76, v8, v9\n\t" \
    "v_cvt_pk_bf16_f32 v77, v10, v11\n\t" \
    "v_cvt_pk_bf16_f32 v78, v12, v13\n\t" \
    "v_cvt_pk_bf16_f32 v79, v14, v15\n\t" \
    "s_waitcnt lgkmcnt(0)\n\t" \
    "v_mfma_f32_32x32x16_bf16 v[16:31], v[184:187], v[48:51], v[168:183]\n\t" \
    "v_exp_f32 v96, v96\n\t" \
    "v_exp_f32 v97, v97\n\t" \
    "v_exp_f32 v98, v98\n\t" \
    "v_exp_f32 v99, v99\n\t" \
    "v_mfma_f32_32x32x16_bf16 v[32:47], v[64:67], v[72:75], v[32:47]\n\t" \
    "v_exp_f32 v100, v100\n\t" \
    "v_exp_f32 v101, v101\n\t" \
    "v_exp_f32 v102, v102\n\t" \
    "v_exp_f32 v103, v103\n\t" \
    "v_cvt_pk_bf16_f32 v152, v96, v97\n\t" \
    "v_cvt_pk_bf16_f32 v153, v98, v99\n\t" \
    "v_mfma_f32_32x32x16_bf16 v[16:31], v[188:191], v[52:55], v[16:31]\n\t" \
    "ds_read_b128 v[56:59], v80 offset:16384\n\t" \
    "ds_read_b128 v[60:63], v80 offset:17408\n\t" \
    "v_exp_f32 v104, v104\n\t" \
    "v_exp_f32 v105, v105\n\t" \
    "v_exp_f32 v106, v106\n\t" \
    "v_exp_f32 v107, v107\n\t" \
    "v_cvt_pk_bf16_f32 v154, v100, v101\n\t" \
    "v_cvt_pk_bf16_f32 v155, v102, v103\n\t" \
    "v_mfma_f32_32x32x16_bf16 v[32:47], v[68:71], v[76:79], v[32:47]\n\t" \
    "ds_read_b128 v[192:195], v80 offset:14336\n\t" \
    "ds_read_b128 v[196:199], v80 offset:15360\n\t" \
    "v_exp_f32 v108, v108\n\t" \
    "v_exp_f32 v109, v109\n\t" \
    "v_exp_f32 v110, v110\n\t" \
    "v_exp_f32 v111, v111\n\t" \
    "v_cvt_pk_bf16_f32 v156, v104, v105\n\t" \
    "v_cvt_pk_bf16_f32 v157, v106, v107\n\t" \
    "v_cvt_pk_bf16_f32 v158, v108, v109\n\t" \
    "v_cvt_pk_bf16_f32 v159, v110, v111\n\t" \
    "s_waitcnt lgkmcnt(0)\n\t" \
    "v_mfma_f32_32x32x16_bf16 v[112:127], v[184:187], v[144:147], 0\n\t" \
    "v_exp_f32 v16, v16\n\t" \
    "v_exp_f32 v17, v17\n\t" \
    "v_exp_f32 v18, v18\n\t" \
    "v_exp_f32 v19, v19\n\t" \
    "v_mfma_f32_32x32x16_bf16 v[128:143], v[64:67], v[152:155], v[128:143]\n\t" \
    "v_exp_f32 v20, v20\n\t" \
    "v_exp_f32 v21, v21\n\t" \
    "v_exp_f32 v22, v22\n\t" \
    "v_exp_f32 v23, v23\n\t" \
    "v_cvt_pk_bf16_f32 v84, v16, v17\n\t" \
    "v_cvt_pk_bf16_f32 v85, v18, v19\n\t" \
    "v_mfma_f32_32x32x16_bf16 v[112:127], v[188:191], v[148:151], v[112:127]\n\t" \
    "v_exp_f32 v24, v24\n\t" \
    "v_exp_f32 v25, v25\n\t" \
    "v_exp_f32 v26, v26\n\t" \
    "v_exp_f32 v27, v27\n\t" \
    "v_cvt_pk_bf16_f32 v86, v20, v21\n\t" \
    "v_cvt_pk_bf16_f32 v87, v22, v23\n\t" \
    "v_mfma_f32_32x32x16_bf16 v[128:143], v[68:71], v[156:159], v[128:143]\n\t" \
    "v_exp_f32 v28, v28\n\t" \
    "v_exp_f32 v29, v29\n\t" \
    "v_exp_f32 v30, v30\n\t" \
    "v_exp_f32 v31, v31\n\t" \
    "v_cvt_pk_bf16_f32 v88, v24, v25\n\t" \
    "v_cvt_pk_bf16_f32 v89, v26, v27\n\t" \
    "v_cvt_pk_bf16_f32 v90, v28, v29\n\t" \
    "v_cvt_pk_bf16_f32 v91, v30, v31\n\t" \
    "s_waitcnt lgkmcnt(0)\n\t" \
    "v_mfma_f32_32x32x16_bf16 v[96:111], v[56:59], v[144:147], v[168:183]\n\t" \
    "v_exp_f32 v112, v112\n\t" \
    "v_exp_f32 v113, v113\n\t" \
    "v_exp_f32 v114, v114\n\t" \
    "v_exp_f32 v115, v115\n\t" \
    "v_mfma_f32_32x32x16_bf16 v[32:47], v[192:195], v[84:87], v[32:47]\n\t" \
    "v_exp_f32 v116, v116\n\t" \
    "v_exp_f32 v117, v117\n\t" \
    "v_exp_f32 v118, v118\n\t" \
    "v_exp_f32 v119, v119\n\t" \
    "v_cvt_pk_bf16_f32 v160, v112, v113\n\t" \
    "v_cvt_pk_bf16_f32 v161, v114, v115\n\t" \
    "v_mfma_f32_32x32x16_bf16 v[96:111], v[60:63], v[148:151], v[96:111]\n\t" \
    "v_exp_f32 v120, v120\n\t" \
    "v_exp_f32 v121, v121\n\t" \
    "v_exp_f32 v122, v122\n\t" \
    "v_exp_f32 v123, v123\n\t" \
    "v_cvt_pk_bf16_f32 v162, v116, v117\n\t" \
    "v_cvt_pk_bf16_f32 v163, v118, v119\n\t" \
    "v_mfma_f32_32x32x16_bf16 v[32:47], v[196:199], v[88:91], v[32:47]\n\t" \
    "ds_read_b128 v[64:67], v80 offset:18432\n\t" \
    "ds_read_b128 v[68:71], v80 offset:19456\n\t" \
    "v_exp_f32 v124, v124\n\t" \
    "v_exp_f32 v125, v125\n\t" \
    "v_exp_f32 v126, v126\n\t" \
    "v_exp_f32 v127, v127\n\t" \
    "v_cvt_pk_bf16_f32 v164, v120, v121\n\t" \
    "v_cvt_pk_bf16_f32 v165, v122, v123\n\t" \
    "v_cvt_pk_bf16_f32 v166, v124, v125\n\t" \
    "v_cvt_pk_bf16_f32 v167, v126, v127\n\t" \
    "s_waitcnt lgkmcnt(0)\n\t" \
    "v_exp_f32 v96, v96\n\t" \
    "v_exp_f32 v97, v97\n\t" \
    "v_exp_f32 v98, v98\n\t" \
    "v_exp_f32 v99, v99\n\t" \
    "v_mfma_f32_32x32x16_bf16 v[128:143], v[192:195], v[160:163], v[128:143]\n\t" \
    "v_exp_f32 v100, v100\n\t" \
    "v_exp_f32 v101, v101\n\t" \
    "v_exp_f32 v102, v102\n\t" \
    "v_exp_f32 v103, v103\n\t" \
    "v_cvt_pk_bf16_f32 v152, v96, v97\n\t" \
    "v_cvt_pk_bf16_f32 v153, v98, v99\n\t" \
    "v_exp_f32 v104, v104\n\t" \
    "v_exp_f32 v105, v105\n\t" \
    "v_exp_f32 v106, v106\n\t" \
    "v_exp_f32 v107, v107\n\t" \
    "v_cvt_pk_bf16_f32 v154, v100, v101\n\t" \
    "v_cvt_pk_bf16_f32 v155, v102, v103\n\t" \
    "v_mfma_f32_32x32x16_bf16 v[128:143], v[196:199], v[164:167], v[128:143]\n\t" \
    "v_exp_f32 v108, v108\n\t" \
    "v_exp_f32 v109, v109\n\t" \
    "v_exp_f32 v110, v110\n\t" \
    "v_exp_f32 v111, v111\n\t" \
    "v_cvt_pk_bf16_f32 v156, v104, v105\n\t" \
    "v_cvt_pk_bf16_f32 v157, v106, v107\n\t" \
    "v_cvt_pk_bf16_f32 v158, v108, v109\n\t" \
    "v_cvt_pk_bf16_f32 v159, v110, v111\n\t" \
    "s_waitcnt lgkmcnt(0)\n\t" \
    "s_nop 0\n\t" \
    "v_mfma_f32_32x32x16_bf16 v[128:143], v[64:67], v[152:155], v[128:143]\n\t" \
    "v_mfma_f32_32x32x16_bf16 v[128:143], v[68:71], v[156:159], v[128:143]\n\t" \
    "s_nop 7\n\t" \
    "s_nop 7\n\t" \
    ""
struct Ctx {
    const uint16_t *xA, *wA, *woA, *xB, *wB, *woB; uint16_t* out; char* ws;
    unsigned lds0, lane, head, qb, g, j;
    unsigned tb, cgB, cgA;
};
template <int N> DI void waitv() { asm volatile("s_waitcnt vmcnt(%0)" :: "i"(N) : "memory"); }
#define SBAR() __builtin_amdgcn_sched_barrier(0)

struct WoFrag { f32x16 f[8]; };
struct WoAddr { unsigned a0, a1; };
DI WoAddr outproj_wo_addr(unsigned lwo, unsigned w4, unsigned lane) {
    const unsigned h = lane >> 5, kq = w4 >> 1, q4 = (lane & 15) >> 2, p4 = lane & 3, blk = (lane >> 4) & 1;
    const unsigned lb0 = 32 * blk + 8 * p4, lb1 = 64 + lb0, sw = ((q4 >> 1) & 1) << 2, rowb = lwo + (8 * kq * 28 + 4 * h + q4) * 128;
    return WoAddr{rowb + ((((lb0 >> 4) ^ sw) << 4) | (lb0 & 15)), rowb + ((((lb1 >> 4) ^ sw) << 4) | (lb1 & 15))};
}
DI void outproj_load_wo(WoFrag& wf, WoAddr wa) {
    asm volatile(OUTPROJ_WO : "=&{v[64:79]}"(wf.f[0]), "=&{v[80:95]}"(wf.f[1]), "=&{v[96:111]}"(wf.f[2]), "=&{v[112:127]}"(wf.f[3]),
                              "=&{v[128:143]}"(wf.f[4]), "=&{v[144:159]}"(wf.f[5]), "=&{v[160:175]}"(wf.f[6]), "=&{v[176:191]}"(wf.f[7])
                 : [wa0] "v"(wa.a0), [wa1] "v"(wa.a1) : "memory");
}
#define V8(a) "v" #a "0","v" #a "1","v" #a "2","v" #a "3","v" #a "4","v" #a "5","v" #a "6","v" #a "7","v" #a "8","v" #a "9"
#define OP_CLOB "v0","v1","v2","v3","v4","v5","v6","v7","v8","v9",V8(1),V8(2),V8(3),V8(4),V8(5),"v60","v61","v62","v63", \
    "v192","v193","v194","v195","v196","v197","v198","v199",V8(20),V8(21),"v220","v221","v222","v223","v224","v225","v226","v227", "vcc", "scc", "memory"
#define DBG_DECL
#define DBG_OUT
#define DBG_SAVE()
struct OpArgs { __amdgpu_buffer_rsrc_t rO; unsigned so, kq; const char* fl; uint16_t* orow; };
DI OpArgs outproj_args(const Ctx& c, char* ws, unsigned br, unsigned w4) {
    const unsigned lane = c.lane, r = lane & 31, h = lane >> 5;
    const unsigned tt = w4 & 1, kq = rfl(w4 >> 1), tb64 = 4 * c.g + (c.j & 3), cg = c.j >> 2, tile = 2 * tb64 + tt;
    return OpArgs{mk_rsrc(ws + WS_O, 4u << 20), rfl(((br * 64 + tile) * 16 + 8 * kq) * 2048), kq,
                  ws + WS_FO + ((br * 64 + tile) * 16 + 8 * kq + (lane & 7)) * 128, c.out + (size_t)(tile * 32 + r) * 896 + br * 448 + cg * 64 + kq * 32 + 8 * h};
}
DI void outproj_run(const Ctx& c, const OpArgs& a, const WoFrag& wf, u64 tag, unsigned arr, unsigned scr_mine, unsigned scr_peer, unsigned flag_mine, unsigned flag_peer STAMP_ARG) {
    unsigned iss, pend, cap, t0, nxt, lnd; DBG_DECL;
    asm volatile(OUTPROJ_RUN : [iss] "=&s"(iss), [pend] "=&s"(pend), [cap] "=&s"(cap), [t0] "=&s"(t0), [nxt] "=&s"(nxt), [lnd] "=&s"(lnd) DBG_OUT
                 : "{v[64:79]}"(wf.f[0]), "{v[80:95]}"(wf.f[1]), "{v[96:111]}"(wf.f[2]), "{v[112:127]}"(wf.f[3]), "{v[128:143]}"(wf.f[4]), "{v[144:159]}"(wf.f[5]), "{v[160:175]}"(wf.f[6]), "{v[176:191]}"(wf.f[7]),
                   [fl] "v"(a.fl), [tag] "s"(tag), [so] "s"(a.so), [vl] "v"(c.lane * 16), [srd] "s"(a.rO), [kq] "s"(a.kq), [sm] "v"(scr_mine + c.lane * 16), [sp] "v"(scr_peer + c.lane * 16),
                   [fm] "v"(flag_mine), [fp] "v"(flag_peer), [out] "v"(a.orow), [arr] "v"(arr)
                 : OP_CLOB);
    DBG_SAVE();
}
DI void outproj_own(const Ctx& c, const OpArgs& a, WoAddr wa, u64 tag, char* pub, unsigned w2, unsigned rd, unsigned scr_mine, unsigned scr_peer, unsigned flag_mine, unsigned flag_peer STAMP_ARG) {
    unsigned iss, pend, cap, t0, nxt, lnd; DBG_DECL;
    asm volatile(OUTPROJ_OWN : [iss] "=&s"(iss), [pend] "=&s"(pend), [cap] "=&s"(cap), [t0] "=&s"(t0), [nxt] "=&s"(nxt), [lnd] "=&s"(lnd) DBG_OUT
                 : [fl] "v"(a.fl), [tag] "s"(tag), [so] "s"(a.so), [vl] "v"(c.lane * 16), [srd] "s"(a.rO), [kq] "s"(a.kq), [sm] "v"(scr_mine + c.lane * 16), [sp] "v"(scr_peer + c.lane * 16),
                   [fm] "v"(flag_mine), [fp] "v"(flag_peer), [out] "v"(a.orow), [pub] "v"(pub), [taglo] "s"((unsigned)tag), [taghi] "s"((unsigned)(tag >> 32)),
                   [wa0] "v"(wa.a0), [wa1] "v"(wa.a1), [w2] "v"(w2), [rd] "v"(rd)
                 : V8(7),V8(8),V8(9),V8(10),V8(11),V8(12),V8(13),V8(14),V8(15),V8(16),V8(17),V8(18),"v64","v65","v66","v67","v68","v69","v190","v191", OP_CLOB);
    DBG_SAVE();
}
DI char* qkvflag(char* ws, unsigned br, unsigned cg, unsigned tb) { return ws + WS_FKV + ((br * 16 + cg) * 16 + tb) * 128; }
DI unsigned unit_off(unsigned br, unsigned U, unsigned tile) {
    return U < 16 ? WS_Q + ((br * 16 + U) * 64 + tile) * 2048 : WS_KV + ((br * 16 + (U & 15)) * 64 + tile) * 4096 + (U < 32 ? 0 : 2048);
}

#define FIN_CLOB "v16","v17","v18","v19","v20","v21","v22","v23","v24","v25","v26","v27","vcc","memory"
template <bool Y, typename SLOW>
DI void finish_tile(const Ctx& c, char* ws, unsigned w4, unsigned w8, unsigned br, unsigned peer, const f32x16& o, float m, SLOW slow) {
    const unsigned lane = c.lane, h = lane >> 5;
    const unsigned peerm = c.lds0 + L_PM + (w8 ^ 2) * 256 + lane * 4, rd = c.lds0 + L_SYNC + SY_RD + 4 * w8, epoch = 1;
    f32x16 e; u64 bad;
    if (Y) asm volatile(FINISH_Y : "=&{v[0:15]}"(e), [bad] "=&s"(bad) : "{v[128:143]}"(o), [m] "v"(m), [pa] "v"(peer + lane * 16), [pm] "v"(peerm),
                        [rd] "v"(rd), [ep] "v"(epoch), [lmin] "s"((unsigned)(127 - MBIASV - 3) << 23) : FIN_CLOB);
    else asm volatile(FINISH_X : "=&{v[0:15]}"(e), [bad] "=&s"(bad) : "{v[32:47]}"(o), [m] "v"(m), [pa] "v"(peer + lane * 16), [pm] "v"(peerm),
                      [rd] "v"(rd), [ep] "v"(epoch), [lmin] "s"((unsigned)(127 - MBIASV - 3) << 23) : FIN_CLOB);
    if (__builtin_expect(bad != 0, 0)) {
        f32x16 os; slow(os);
        auto r2 = __builtin_amdgcn_permlane32_swap(__float_as_uint(os[12]), __float_as_uint(os[12]), false, false);
        const float iv = 1.f / __uint_as_float(r2[1]);
#pragma unroll
        for (int i = 0; i < 16; ++i) e[i] = os[i] * iv;
    }
    const u32x4 f0v = {cvtpk(e[0], e[1]), cvtpk(e[2], e[3]), cvtpk(e[4], e[5]), cvtpk(e[6], e[7])};
    const u32x4 f1v = {cvtpk(e[8], e[9]), cvtpk(e[10], e[11]), h ? 0u : cvtpk(e[12], e[13]), h ? 0u : cvtpk(e[14], e[15])};
    const __amdgpu_buffer_rsrc_t rO = mk_rsrc(ws + WS_O, 4u << 20);
    const unsigned so = ((br * 64 + c.qb * 4 + 2 * (w4 & 1) + (w4 >> 1)) * 16 + c.head) * 2048;
    st128c(f0v, rO, lane * 16, so); st128c(f1v, rO, lane * 16, so + 1024);
}
DI char* otileflag(const Ctx& c, char* ws, unsigned w, unsigned br) { return ws + WS_FO + ((br * 64 + c.qb * 4 + 2 * (w & 1) + (w >> 1)) * 16 + c.head) * 128; }
#define ATT_CLOB_X "v16","v17","v18","v19","v20","v21","v22","v23","v24","v25","v26","v27","v28","v29","v30","v31"
#define ATT_CLOB_P "v72","v73","v74","v75","v76","v77","v78","v79","v84","v85","v86","v87","v88","v89","v90","v91","v152","v153","v154","v155","v156","v157","v158","v159","v160","v161","v162","v163","v164","v165","v166","v167"
#define ATT_CLOB_Y "v112","v113","v114","v115","v116","v117","v118","v119","v120","v121","v122","v123","v124","v125","v126","v127"
#define ATT_A_RUN(BLOCK) asm volatile("s_nop 1\n\t" BLOCK \
    : "=&{v[32:47]}"(oX), "=&{v[128:143]}"(oY) \
    : "{v[48:51]}"(qX0), "{v[52:55]}"(qX1), "{v[144:147]}"(qY0), "{v[148:151]}"(qY1), "{v80}"(base), "{v[168:183]}"(msk) \
    : "v0","v1","v2","v3","v4","v5","v6","v7","v8","v9","v10","v11","v12","v13","v14","v15", ATT_CLOB_X, \
      "v96","v97","v98","v99","v100","v101","v102","v103","v104","v105","v106","v107","v108","v109","v110","v111", ATT_CLOB_Y, ATT_CLOB_P, \
      "v56","v57","v58","v59","v60","v61","v62","v63","v64","v65","v66","v67","v68","v69","v70","v71", \
      "v184","v185","v186","v187","v188","v189","v190","v191","v192","v193","v194","v195","v196","v197","v198","v199", "memory")

struct Hlp { unsigned vx0, vx1, vw0, vw1, vw2, hw; };
DI void p1_issue(const Ctx& c, const Hlp& hp, __amdgpu_buffer_rsrc_t rx, __amdgpu_buffer_rsrc_t rw, unsigned sw, unsigned kc, unsigned slot) {
    const unsigned dst = c.lds0 + slot * P1_SLOT, sx = (c.tb * 128 + 32 * hp.hw) * 896 + kc * 128, dx = dst + hp.hw * 4096;
    dma16(dx, hp.vx0, rx, sx); dma16(dx + 1024, hp.vx1, rx, sx + 8 * 896); dma16(dx + 2048, hp.vx0, rx, sx + 16 * 896); dma16(dx + 3072, hp.vx1, rx, sx + 24 * 896);
    const unsigned so = sw + kc * 64 * 2688, dw = dst + 16384 + hp.hw * 3072;
    dma16(dw, hp.vw0, rw, so); dma16(dw + 1024, hp.vw1, rw, so); dma16(dw + 2048, hp.vw2, rw, so);
}
#define HP_FRAG 0
#define HP_SYNC 3
#define HP_RUN 0
#define INPF_SLEEP 8
constexpr int P1_PUB = 10;
DI void helper_start(const Ctx& c, unsigned hw, Hlp& hp, u64& ev) {
    const unsigned lane = c.lane;
    const __amdgpu_buffer_rsrc_t rxB = mk_rsrc(c.xB, S_ * D_ * 2), rwB = mk_rsrc(c.wB, D_ * 3 * D_ * 2);
    hp.vx0 = (lane >> 3) * 896 + (((lane & 7) ^ ((lane >> 4) & 7)) << 4);
    hp.vx1 = (lane >> 3) * 896 + (((lane & 7) ^ ((4 + (lane >> 4)) & 7)) << 4);
    {
        unsigned v[3];
#pragma unroll
        for (int i = 0; i < 3; ++i) { const unsigned idx = 64 * i + lane, row = (idx * 43691u) >> 19, ch = idx - 12 * row; v[i] = ch == 11 ? 0x80000000u : row * 2688 + ch * 16; }
        hp.vw0 = v[0]; hp.vw1 = v[1]; hp.vw2 = v[2];
    }
    hp.hw = hw;
    const unsigned swB = 16 * hw * 2688 + ((168 * c.cgB) & ~15u);
#pragma unroll 1
    for (unsigned n = 0; n < 4; ++n) p1_issue(c, hp, rxB, rwB, swB, n, n + 1);
    asm volatile("global_load_dwordx2 %0, %1, off sc1" : "=&v"(ev) : "v"(c.ws + WS_E) : "memory");
}
DI void helper_prog(const Ctx& c, unsigned wid, const Hlp& hp, u64 ev) {
    char* ws = c.ws; const unsigned lane = c.lane, hw = wid - 4, lds0 = c.lds0;
    STAMP_DECL;
    STAMP(0);
    const __amdgpu_buffer_rsrc_t rxB = mk_rsrc(c.xB, S_ * D_ * 2), rwB = mk_rsrc(c.wB, D_ * 3 * D_ * 2);
    const unsigned swB = 16 * hw * 2688 + ((168 * c.cgB) & ~15u), swA = 16 * hw * 2688 + ((168 * c.cgA) & ~15u);
    const __amdgpu_buffer_rsrc_t rxA = mk_rsrc(c.xA, S_ * D_ * 2), rwA = mk_rsrc(c.wA, D_ * 3 * D_ * 2);
    const unsigned qh = hw & 1, kh = hw >> 1;
    f32x16 msk;
#pragma unroll
    for (int i = 0; i < 16; ++i) { const unsigned key = (i & 3) + 8 * (i >> 2) + 4 * (lane >> 5); msk[i] = ((key > (lane & 31)) == (kh == 0)) ? 0.f : -INFINITY; }
    asm volatile("" : "+{v[168:183]}"(msk));
    unsigned slot = 0;
#pragma unroll 1
    for (unsigned n = 0; n < 3; ++n) {
        WAITV(21); BAR();
        if (n == 0) STAMP(1); if (n == 1) STAMP(2); if (n == 2) STAMP(3);
        p1_issue(c, hp, rxB, rwB, swB, n + 4, slot); slot = slot == 4 ? 0 : slot + 1;
    }
#pragma unroll 1
    for (unsigned n = 3; n < 8; ++n) {
        WAITV(21); BAR();
        if (n == 3) STAMP(5); if (n == 4) STAMP(6); if (n == 5) STAMP(7); if (n == 6) STAMP(8); if (n == 7) STAMP(10);
        p1_issue(c, hp, rxA, rwA, swA, n - 3, slot); slot = slot == 4 ? 0 : slot + 1;
    }
    asm volatile("" : "+v"(ev));
    const u64 tag = mix64(rfl64(ev));
    WAITV(21); BAR();
    STAMP(11);
    WAITV(14); BAR();
    STAMP(12);
    WAITV(7); BAR();
    if (hw == 0 && lane == 0) set_flag(qkvflag(ws, 1, c.cgB, c.tb), tag);
    p1_issue(c, hp, rxA, rwA, swA, 5, 3); p1_issue(c, hp, rxA, rwA, swA, 6, 4);
    WAITV(14); BAR();
    WAITV(7); BAR();
    STAMP(14);
    const __amdgpu_buffer_rsrc_t rWS = mk_rsrc(ws, 16u << 20);
    const unsigned vl = lane * 16;
    auto issueB = [&](unsigned s, unsigned slot) {
        const unsigned dst = lds0 + slot * 32768 + hw * 8192, so = WS_KV + (16 + c.head) * 64 * 4096 + s * 32768 + hw * 8192;
#pragma unroll
        for (int i = 0; i < 8; ++i) dma16c(dst + i * 1024, vl, rWS, so + i * 1024);
    };
    auto issueQ = [&](unsigned br) {
        const unsigned so = WS_Q + ((br * 16 + c.head) * 64 + c.qb * 4 + hw) * 2048, dst = lds0 + L_Q + hw * 2048;
        dma16c(dst, vl, rWS, so); dma16c(dst + 1024, vl, rWS, so + 1024);
    };
    const unsigned cq = c.head / 3, ck = (16 + c.head) / 3, cv = (32 + c.head) / 3;
    poll_flags(lane < 16 ? qkvflag(ws, 1, ck, lane) : lane < 32 ? qkvflag(ws, 1, cv, lane - 16) : qkvflag(ws, 1, cq, c.qb), tag);
    STAMP(9);
    BAR();
    issueQ(1); issueB(0, 0); issueB(1, 1);
    const bool act = c.j < 28;
    const unsigned cg = c.j >> 2;
    unsigned vwo = (lane >> 3) * 896 + (((lane & 7) ^ (((lane >> 4) & 1) << 2)) << 4);
    OpArgs opa = outproj_args(c, ws, 0, hw); WoAddr wa = outproj_wo_addr(lds0 + L_WO, hw, lane);
    asm volatile("" : "+v"(vwo), "+v"(opa.fl), "+v"(opa.orow), "+v"(wa.a0), "+v"(wa.a1));
    STAMP(16);
    WAITV(8); BAR(); STAMP(17);
    u32x4 pa[6];
#pragma unroll
    for (int i = 0; i < 6; ++i) pa[i] = *(const LAS u32x4*)(size_t)(lds0 + L_PARK + hw * 8192 + i * 1024 + vl);
#pragma unroll
    for (int i = 0; i < 6; ++i) asm volatile("" : "+v"(pa[i]));
    asm volatile("s_waitcnt lgkmcnt(0)" ::: "memory");
    const unsigned tileA = c.tb * 4 + hw;
    const unsigned ua0 = unit_off(0, 3 * c.cgA, tileA), ua1 = unit_off(0, 3 * c.cgA + 1, tileA), ua2 = unit_off(0, 3 * c.cgA + 2, tileA);
    st128c(pa[0], rWS, vl, ua0); st128c(pa[1], rWS, vl, ua0 + 1024); st128c(pa[2], rWS, vl, ua1);
    st128c(pa[3], rWS, vl, ua1 + 1024); st128c(pa[4], rWS, vl, ua2); st128c(pa[5], rWS, vl, ua2 + 1024);
    WAITV(0); BAR();
    if (hw == 0 && lane == 0) set_flag(qkvflag(ws, 0, c.cgA, c.tb), tag);
    issueB(2, 2);
    { const unsigned lo = c.qb < 2 ? 0 : c.qb - 2, n = c.qb - lo + 1;
      poll_flags(lane < n ? qkvflag(ws, 0, ck, lo + lane) : lane < 2 * n ? qkvflag(ws, 0, cv, lo + lane - n) : qkvflag(ws, 0, cq, c.qb), tag); }
    BAR();
    STAMP(32);
    issueB(3, 0);
    issueQ(0);
#pragma unroll
    for (int u = 0; u < 3; ++u) {
        const int it = 3 * (int)hw + u, t = (int)c.qb * 4 - 8 + it;
        const unsigned so = t < 0 ? 0x80000000u : WS_KV + c.head * 64 * 4096 + (unsigned)t * 4096, dst = lds0 + L_AKV + it * 4096;
#pragma unroll
        for (int i = 0; i < 4; ++i) dma16c(dst + i * 1024, vl, rWS, so + i * 1024);
    }
    WAITV(14); BAR();
    STAMP(33);
    issueB(4, 1);
    WAITV(8); lds_arrive(lds0 + L_SYNC + SY_AL, lane); lds_wait_ge_sleep(lds0 + L_SYNC + SY_AL, 4);
    STAMP(34);
    const int T = (int)c.qb * 4 + 2 * (int)qh;
    f32x16 oX, oY; float mX, mY;
    {
        const int t0 = kh ? T - 3 : T - 8, tp = kh ? T : T - 4;
        const unsigned base = lds0 + L_AKV + vl + (unsigned)(t0 - ((int)c.qb * 4 - 8)) * 4096;
        bf16x8 qX0, qX1, qY0, qY1;
        asm volatile(ATTN_A_PROBE
                     : "=&{v[48:51]}"(qX0), "=&{v[52:55]}"(qX1), "=&{v[144:147]}"(qY0), "=&{v[148:151]}"(qY1), [mX] "=&v"(mX), [mY] "=&v"(mY)
                     : [qa] "v"(lds0 + L_Q + 2 * qh * 2048 + vl), [pp] "v"(base + (unsigned)(tp - t0) * 4096), "{v[168:183]}"(msk), [kh] "s"(kh), [bias] "s"(MBIAS), [hi] "s"(0xffffffff00000000ull)
                     : "v0","v1","v2","v3","v4","v5","v6","v7","v8","v9","v10","v11","v12","v13","v14","v15",
                       "v96","v97","v98","v99","v100","v101","v102","v103","v104","v105","v106","v107","v108","v109","v110","v111",
                       "v184","v185","v186","v187","v188","v189","v190","v191","v192","v193","v194","v195","v196","v197", "scc", "memory");
        if (kh == 0) ATT_A_RUN(ATTN_A0); else ATT_A_RUN(ATTN_A1);
        if (tp < 0) { mX = -INFINITY; mY = -INFINITY; }
    }
    STAMP(36);
    WAITV(0); BAR();
    STAMP(35);
    issueB(5, 2);
    auto issueWo = [&](const uint16_t* wo, unsigned i0, unsigned i1) {
        const __amdgpu_buffer_rsrc_t rw = mk_rsrc(wo, D_ * D_ * 2);
#pragma unroll 1
        for (unsigned i = i0 + hw; i < i1; i += 4) dma16(lds0 + L_WO + i * 1024, vwo, rw, i * 8 * 896 + cg * 128);
    };
    if (act) issueWo(c.woA, 16, 56);
    {
        unsigned cap, t0; const unsigned pk = lds0 + L_AKV + hw * 4096 + vl, pm = lds0 + L_PM + (4 + hw) * 256 + lane * 4;
        const unsigned cm = lds0 + L_SYNC + SY_CMB + 4 * (4 + hw), cp = lds0 + L_SYNC + SY_CMB + 4 * (4 + (hw ^ 2));
        if (kh) asm volatile(PARK_X : [cap] "=&s"(cap), [t0] "=&s"(t0) : "{v[32:47]}"(oX), [pa] "v"(pk), [pm] "v"(pm), [ms] "v"(mX), [cm] "v"(cm), [cp] "v"(cp) : "v0", "v1", "scc", "memory");
        else asm volatile(PARK_Y : [cap] "=&s"(cap), [t0] "=&s"(t0) : "{v[128:143]}"(oY), [pa] "v"(pk), [pm] "v"(pm), [ms] "v"(mY), [cm] "v"(cm), [cp] "v"(cp) : "v0", "v1", "scc", "memory");
        const int Tf = T + (int)kh, lo = Tf < 8 ? 0 : Tf - 8;
        const __amdgpu_buffer_rsrc_t rKV = mk_rsrc(ws + WS_KV, 8u << 20);
        auto slowA = [&](f32x16& os) { float ms; const __amdgpu_buffer_rsrc_t rQ = mk_rsrc(ws + WS_Q, 4u << 20); const unsigned so = (c.head * 64 + Tf) * 2048;
            slow_attn(rKV, c.head * 64 * 4096, __builtin_bit_cast(bf16x8, ld128c(rQ, lane * 16, so)), __builtin_bit_cast(bf16x8, ld128c(rQ, lane * 16, so + 1024)), lo, Tf - lo + 1, false, Tf >= 8 ? 2 : 0, 1, lane, os, ms); };
        const unsigned peer = lds0 + L_AKV + (hw ^ 2) * 4096;
        if (kh) finish_tile<true>(c, ws, hw, 4 + hw, 0, peer, oY, mY, slowA); else finish_tile<false>(c, ws, hw, 4 + hw, 0, peer, oX, mX, slowA);
    }
    STAMP(37);
    if (act) WAITV(12); else WAITV(2);
    BAR();
    STAMP(38);
    WAITV(0);
    if (lane == 0) set_flag(otileflag(c, ws, hw, 0), tag);
    issueB(6, 0);
    if (act) issueWo(c.woA, 0, 16);
    WAITV(0); BAR();
    STAMP(39);
    issueB(7, 1);
    WoFrag wf;
    if (act) {
        __builtin_amdgcn_s_setprio(HP_FRAG);
        outproj_load_wo(wf, wa);
        __builtin_amdgcn_s_setprio(HP_SYNC);
        STAMP(23);
    }
    WAITV(0); BAR();
    STAMP(18);
    if (blockIdx.x == 255 && hw == 1) {
#pragma unroll 1
        for (unsigned q = 0; q < 4; ++q) poll_flags(ws + WS_FKV + (256 + q * 64 + lane) * 128, tag);
        if (lane == 0) set_flag(ws + WS_E, tag);
    }
    if (!act) { STAMP_FLUSH(); return; }
    issueWo(c.woB, 0, 56);
    STAMP(19);
    __builtin_amdgcn_s_setprio(HP_RUN);
    outproj_run(c, opa, wf, tag, lds0 + L_SYNC + SY_W2, lds0 + L_HSCR + hw * 4096, lds0 + L_HSCR + (hw ^ 2) * 4096, lds0 + L_SYNC + SY_RED + 4 * (4 + hw), lds0 + L_SYNC + SY_RED + 4 * (4 + (hw ^ 2)) STAMP_PASS);
    STAMP(21);
    STAMP_FLUSH();
    asm volatile(".Lfused_code_end:");
}

#define P1_CLOB "v48","v49","v50","v51","v52","v53","v54","v55","v56","v57","v58","v59","v60","v61","v62","v63","v64","v65","v66","v67","v68","v69","v70","v71","v72","v73","v74","v75","v76","v77","v78","v79", \
    "v88","v89","v90","v91","v92","v93","v94","v95","v96","v97","v98","v99","v100","v101","v102","v103","v104","v105","v106","v107","v108","v109","v110","v111","v112","v113","v114","v115","v116","v117","v118","v119"
#define P1_ACC "v0","v1","v2","v3","v4","v5","v6","v7","v8","v9","v10","v11","v12","v13","v14","v15","v16","v17","v18","v19","v20","v21","v22","v23","v24","v25","v26","v27","v28","v29","v30","v31", \
    "v32","v33","v34","v35","v36","v37","v38","v39","v40","v41","v42","v43","v44","v45","v46","v47"
#define P1_RUN(BLOCK) asm volatile("s_waitcnt lgkmcnt(0)\n\t" BLOCK \
    : "+{v80}"(xa0), "+{v81}"(xa1), "+{v82}"(xa2), "+{v83}"(xa3), "+{v84}"(wa), [cnt] "+s"(cnt), [slot] "+s"(slot), [dl] "=&s"(dl), [t0] "=&s"(t0), [t1] "=&s"(t1), \
      "+{v[200:203]}"(p0), "+{v[204:207]}"(p1), "+{v[208:211]}"(p2), "+{v[212:215]}"(p3), "+{v[216:219]}"(p4), "+{v[220:223]}"(p5), "+v"(inpf) \
    : [wv] "s"(wv), [c0] "s"(c0), "{v224}"(vl), [srd] "s"(rWS), [so0] "s"(so0), [so1] "s"(so1), [so2] "s"(so2), [qm] "s"(qm), [hi] "s"(0xffffffff00000000ull), \
      [r28] "s"(0xf0000000f0000000ull), [vk] "v"(vk) : P1_ACC, P1_CLOB, "scc", "memory")

DI void compute_prog(const Ctx& c, unsigned w, unsigned codepf) {
    char* ws = c.ws; const unsigned lane = c.lane, r = lane & 31, h = lane >> 5, lds0 = c.lds0;
    STAMP_DECL;
    STAMP(0);
    const u64 evl = __hip_atomic_load((const GAS u64*)(ws + WS_E), __ATOMIC_RELAXED, __HIP_MEMORY_SCOPE_AGENT);
    if (w == 0) *(LAS u32x2*)(size_t)(lds0 + L_SYNC + lane * 8) = u32x2{0u, 0u};
    asm volatile("s_waitcnt vmcnt(0)" : "+v"(codepf) :: "memory");
    const u64 tag = mix64(rfl64(evl));
    unsigned inpf = 0;
    if (w < 2) {
        const unsigned L = (blockIdx.x * 2 + w) * 64 + lane;
        const char* p = L < 14336 ? (const char*)c.xA + L * 128 : L < 23744 ? (const char*)c.wA + (L - 14336) * 128
                      : L < 26880 ? (const char*)c.woB + (L - 23744) * 128 : (const char*)c.woA + (L - 26880) * 128;
        if (L < 30016) asm volatile("global_load_dword %0, %1, off" : "=&v"(inpf) : "v"(p) : "memory");
    }
    {
        unsigned xa0 = lds0 + P1_SLOT + (w * 32 + r) * 128 + (((0 + h) ^ ((r >> 1) & 7)) << 4), xa1 = lds0 + P1_SLOT + (w * 32 + r) * 128 + (((2 + h) ^ ((r >> 1) & 7)) << 4);
        unsigned xa2 = lds0 + P1_SLOT + (w * 32 + r) * 128 + (((4 + h) ^ ((r >> 1) & 7)) << 4), xa3 = lds0 + P1_SLOT + (w * 32 + r) * 128 + (((6 + h) ^ ((r >> 1) & 7)) << 4);
        unsigned wa = lds0 + P1_SLOT + 16384 + (8 * h + ((lane & 15) >> 2)) * 192 + 32 * ((lane >> 4) & 1) + 8 * (lane & 3) + ((168 * c.cgB) & 15);
        unsigned slot = 1; const unsigned vl = lane * 16, tile = c.tb * 4 + w;
        const __amdgpu_buffer_rsrc_t rWS = mk_rsrc(ws, 16u << 20);
        const unsigned so0 = rfl(unit_off(1, 3 * c.cgB, tile)), so1 = rfl(unit_off(1, 3 * c.cgB + 1, tile)), so2 = rfl(unit_off(1, 3 * c.cgB + 2, tile));
        u32x4 p0 = {0u, 0u, 0u, 0u}, p1 = p0, p2 = p0, p3 = p0, p4 = p0, p5 = p0;
        const unsigned vk = r == 28 ? 0x3f803f80u : 0u;
#pragma unroll 1
        for (unsigned st = 0; st < 2; ++st) {
            const unsigned cg = rfl(st ? c.cgA : c.cgB), wv = st ? 14 - P1_PUB : 0xffffffffu, c0 = st ? 6 : 0xfffffff0u;
            const unsigned qm = rfl(cg < 5 ? 7u : cg == 5 ? 1u : 0u);
            unsigned cnt = 7, dl, t0, t1; slot = rfl(slot);
            if (cg < 10) P1_RUN(P1_WWW); else if (cg == 10) P1_RUN(P1_WWX); else P1_RUN(P1_XXX);
            if (st) STAMP(12); else STAMP(5);
            wa += ((168 * c.cgA) & 15) - ((168 * c.cgB) & 15);
        }
        asm volatile("s_waitcnt vmcnt(0)" : "+v"(inpf) :: "memory");
        const unsigned pk = lds0 + L_PARK + w * 8192 + vl;
        ldsw128(pk, p0); ldsw128(pk + 1024, p1); ldsw128(pk + 2048, p2); ldsw128(pk + 3072, p3); ldsw128(pk + 4096, p4); ldsw128(pk + 5120, p5);
    }
    STAMP(14);
    const unsigned qh = w & 1, kh = w >> 1;
    u32x4 zv0 = {0u, 0u, 0u, 0u}, zv1 = zv0, zx0 = zv0, zx1 = zv0, zy0 = zv0, zy1 = zv0;
    asm volatile("" : "+{v[64:67]}"(zv0), "+{v[68:71]}"(zv1), "+{v[84:87]}"(zx0), "+{v[88:91]}"(zx1), "+{v[160:163]}"(zy0), "+{v[164:167]}"(zy1));
    const OpArgs opa = outproj_args(c, ws, 1, w); const WoAddr wa = outproj_wo_addr(lds0 + L_WO, w, lane);
    BAR();
    STAMP(16);
    f32x16 oX = f32x16{}, oY = f32x16{}; float mX, mY;
    {
        const unsigned qa = lds0 + L_Q + 2 * qh * 2048 + lane * 16;
        unsigned kb = lds0 + kh * 16384 + lane * 16, kn = kb + 32768;
        bf16x8 qX0, qX1, qY0, qY1, k0, k1; f32x16 sX, sY;
        asm volatile(ATTN_B_PRO
                     : "=&{v[48:51]}"(qX0), "=&{v[52:55]}"(qX1), "=&{v[144:147]}"(qY0), "=&{v[148:151]}"(qY1), "=&{v[56:59]}"(k0), "=&{v[60:63]}"(k1),
                       "=&{v[0:15]}"(sX), "=&{v[96:111]}"(sY), [mX] "=&v"(mX), [mY] "=&v"(mY)
                     : [qa] "v"(qa), "{v80}"(kb), [bias] "s"(MBIAS), [hi] "s"(0xffffffff00000000ull)
                     : ATT_CLOB_X, ATT_CLOB_Y, "v184","v185","v186","v187","v188","v189","v190","v191","v192","v193","v194","v195","v196","v197", "memory");
        unsigned cnt = 8, slot = 1, dl;
        STAMP(17);
        asm volatile("s_nop 1\n\t" ATTN_B_LOOP
                     : "+{v[32:47]}"(oX), "+{v[128:143]}"(oY), "+{v[0:15]}"(sX), "+{v[96:111]}"(sY), "+{v[56:59]}"(k0), "+{v[60:63]}"(k1), "+{v80}"(kb), "+{v81}"(kn),
                       "+{v[64:67]}"(zv0), "+{v[68:71]}"(zv1), "+{v[84:87]}"(zx0), "+{v[88:91]}"(zx1), "+{v[160:163]}"(zy0), "+{v[164:167]}"(zy1),
                       [cnt] "+s"(cnt), [slot] "+s"(slot), [dl] "=&s"(dl)
                     : "{v[48:51]}"(qX0), "{v[52:55]}"(qX1), "{v[144:147]}"(qY0), "{v[148:151]}"(qY1),
                       [kh] "s"(kh), [pa] "v"(lds0 + L_SCR + w * 4096 + lane * 16), [pm] "v"(lds0 + L_PM + w * 256 + lane * 4), [mX] "v"(mX), [mY] "v"(mY),
                       [cm] "v"(lds0 + L_SYNC + SY_CMB + 4 * w), [cp] "v"(lds0 + L_SYNC + SY_CMB + 4 * (w ^ 2))
                     : ATT_CLOB_X, "v72","v73","v74","v75","v76","v77","v78","v79","v152","v153","v154","v155","v156","v157","v158","v159", ATT_CLOB_Y, "scc", "memory");
    }
    STAMP(18);
    {
        const __amdgpu_buffer_rsrc_t rKV = mk_rsrc(ws + WS_KV, 8u << 20);
        auto slowB = [&](f32x16& os) { float ms; const __amdgpu_buffer_rsrc_t rQ = mk_rsrc(ws + WS_Q, 4u << 20); const unsigned so = ((16 + c.head) * 64 + c.qb * 4 + 2 * qh + kh) * 2048;
            slow_attn(rKV, (16 + c.head) * 64 * 4096, __builtin_bit_cast(bf16x8, ld128c(rQ, lane * 16, so)), __builtin_bit_cast(bf16x8, ld128c(rQ, lane * 16, so + 1024)), 0, 64, false, 0, 0, lane, os, ms); };
        const unsigned peer = lds0 + L_SCR + (w ^ 2) * 4096;
        if (kh) finish_tile<true>(c, ws, w, w, 1, peer, oY, mY, slowB); else finish_tile<false>(c, ws, w, w, 1, peer, oX, mX, slowB);
    }
    STAMP(21);
    if (c.j >= 28) { WAITV(0); if (lane == 0) set_flag(otileflag(c, ws, w, 1), tag); STAMP_FLUSH(); return; }
    outproj_own(c, opa, wa, tag, otileflag(c, ws, w, 1), lds0 + L_SYNC + SY_W2, lds0 + L_SYNC + SY_RD + 4 * (w ^ 2), lds0 + L_SCR + w * 4096, lds0 + L_SCR + (w ^ 2) * 4096,
                lds0 + L_SYNC + SY_RED + 4 * w, lds0 + L_SYNC + SY_RED + 4 * (w ^ 2) STAMP_PASS);
    STAMP(24);
    STAMP_FLUSH();
}

__global__ void __launch_bounds__(512, 2) fused_mha(const uint16_t* __restrict__ xA, const uint16_t* __restrict__ wA, const uint16_t* __restrict__ woA, const uint16_t* __restrict__ xB,
                                                    const uint16_t* __restrict__ wB, const uint16_t* __restrict__ woB, uint16_t* __restrict__ out, char* __restrict__ ws) {
    extern __shared__ __attribute__((aligned(16))) char lds[];
    const unsigned wid = rfl(threadIdx.x >> 6);
    Ctx c; c.xA = xA; c.wA = wA; c.woA = woA; c.xB = xB; c.wB = wB; c.woB = woB; c.out = out; c.ws = ws;
    c.lds0 = (unsigned)(size_t)lds; c.lane = threadIdx.x & 63;
    c.g = blockIdx.x & 7; c.j = blockIdx.x >> 3;
    c.head = 2 * c.g + (c.j >> 4); c.qb = c.j & 15;
    c.tb = 4 * (c.g & 3) + (c.j & 3); c.cgB = 8 * (c.g >> 2) + (c.j >> 2); c.cgA = (c.cgB + 1) & 15;
    Hlp hp; u64 ev = 0; unsigned codepf = 0;
    hp.vx0 = hp.vx1 = hp.vw0 = hp.vw1 = hp.vw2 = hp.hw = 0;
    if (wid >= 4) {
        __builtin_amdgcn_s_setprio(3);
        helper_start(c, wid - 4, hp, ev);
    } else if (wid == 0) {
        u64 pc; unsigned len;
        asm volatile("s_getpc_b64 %0\n.Lfused_code_pc:\n\ts_mov_b32 %1, .Lfused_code_end-.Lfused_code_pc" : "=s"(pc), "=s"(len));
        const unsigned off = ((blockIdx.x >> 3) * 16 + (threadIdx.x & 15)) * 128;
        if ((int)len > 0 && off < len) asm volatile("global_load_dword %0, %1, off" : "=&v"(codepf) : "v"((const char*)pc + off) : "memory");
    }
    asm volatile("" ::: "memory");
    if (wid >= 4) helper_prog(c, wid, hp, ev); else compute_prog(c, wid, codepf);
}

extern "C" void kernel_launch(void* const* d_in, const int* in_sizes, int n_in, void* d_out, int out_size, void* d_ws, size_t ws_size, hipStream_t stream) {
    (void)hipFuncSetAttribute((const void*)fused_mha, hipFuncAttributeMaxDynamicSharedMemorySize, LDS_BYTES);
    fused_mha<<<256, 512, LDS_BYTES, stream>>>((const uint16_t*)d_in[0], (const uint16_t*)d_in[1], (const uint16_t*)d_in[2], (const uint16_t*)d_in[3],
                                                (const uint16_t*)d_in[4], (const uint16_t*)d_in[5], (uint16_t*)d_out, (char*)d_ws);
}
```
